```python
import torch

def _softmax(x, axis=-1):
    return torch.softmax(x, dim=axis)
def _gelu(x):
    return torch.nn.functional.gelu(x, approximate="tanh")
def _silu(x):
    return torch.nn.functional.silu(x)
def _sigmoid(x):
    return torch.sigmoid(x)
def _layernorm(x, axis=-1, eps=1e-5):
    m = x.mean(dim=axis, keepdim=True)
    v = x.var(dim=axis, keepdim=True, unbiased=False)
    return (x - m) / torch.sqrt(v + eps)
def _scatter_add(tgt, idx, src, axis=0):
    return tgt.clone().index_add_(axis, idx, src)
def _segment_sum(src, seg, n):
    out = torch.zeros((n,) + src.shape[1:], dtype=src.dtype)
    return out.index_add_(0, seg, src)
def _pad(x, pw):
    flat = tuple(v for pair in reversed(pw) for v in pair)
    return torch.nn.functional.pad(x, flat)

kernel_name = 'gated_mix__sliding_block_cached__sliding_block_cached__decode'

def setup_inputs():

    _w = (lambda a: (lambda t: t.to(torch.bfloat16) if t.is_floating_point() else t)(torch.as_tensor(a)))
    def _A_setup():
        B, Sq, D = 4, 1, 512
        Hq, Hkv, Sk, Dh = 32, 8, 8192, 128
        assert Hq % Hkv == 0, (Hq, Hkv)
        return {
            "x": _w((lambda *s: torch.randn(*s, dtype=torch.bfloat16) * 0.05)(B, Sq, D)),
            "k_cache": _w((lambda *s: torch.randn(*s, dtype=torch.bfloat16) * 6.4)(B, Hkv, Sk, Dh)),
            "v_cache": _w((lambda *s: torch.randn(*s, dtype=torch.bfloat16) * 0.05)(B, Hkv, Sk, Dh)),
            "wq": _w((lambda *s: torch.randn(*s, dtype=torch.bfloat16) * 6.4)(D, Hq * Dh) * (D ** -0.5)),
            "wo": _w((lambda *s: torch.randn(*s, dtype=torch.bfloat16) * 0.05)(Hq * Dh, D) * ((Hq * Dh) ** -0.5)),
        }
    _A_in = _A_setup()

    def _B_setup():
        B, Sq, D = 4, 1, 512
        Hq, Hkv, Sk, Dh = 32, 8, 8192, 128
        assert Hq % Hkv == 0, (Hq, Hkv)
        return {
            "x": _w((lambda *s: torch.randn(*s, dtype=torch.bfloat16) * 0.05)(B, Sq, D)),
            "k_cache": _w((lambda *s: torch.randn(*s, dtype=torch.bfloat16) * 6.4)(B, Hkv, Sk, Dh)),
            "v_cache": _w((lambda *s: torch.randn(*s, dtype=torch.bfloat16) * 0.05)(B, Hkv, Sk, Dh)),
            "wq": _w((lambda *s: torch.randn(*s, dtype=torch.bfloat16) * 6.4)(D, Hq * Dh) * (D ** -0.5)),
            "wo": _w((lambda *s: torch.randn(*s, dtype=torch.bfloat16) * 0.05)(Hq * Dh, D) * ((Hq * Dh) ** -0.5)),
        }
    _B_in = _B_setup()

    return {
        "A_x": _A_in["x"],
        "A_k_cache": _A_in["k_cache"],
        "A_v_cache": _A_in["v_cache"],
        "A_wq": _A_in["wq"],
        "A_wo": _A_in["wo"],
        "B_x": _B_in["x"],
        "B_k_cache": _B_in["k_cache"],
        "B_v_cache": _B_in["v_cache"],
        "B_wq": _B_in["wq"],
        "B_wo": _B_in["wo"],
        "gate_w": _w((lambda *s: torch.randn(*s, dtype=torch.bfloat16) * 0.05)(512) * 16.0),
    }

def reference(A_x, A_k_cache, A_v_cache, A_wq, A_wo, B_x, B_k_cache, B_v_cache, B_wq, B_wo, gate_w):
    def _A_ref(x, k_cache, v_cache, wq, wo):
        B, Sq, D = x.shape
        Hq, Dh = 32, 128
        Hkv, Sk = k_cache.shape[1], k_cache.shape[2]
        W = min(4096, max(256, Sk // 4))
        rep = Hq // Hkv
        q = (lambda x, *d: x.permute(*d))((x @ wq).reshape(B, Sq, Hq, Dh), 0, 2, 1, 3)
        k = (lambda x, n, axis: torch.repeat_interleave(x, n, dim=axis))(k_cache[:, :, Sk - W:, :], rep, axis=1)
        v = (lambda x, n, axis: torch.repeat_interleave(x, n, dim=axis))(v_cache[:, :, Sk - W:, :], rep, axis=1)
        scores = torch.einsum("bhqd,bhkd->bhqk", q, k) * (Dh ** -0.5)
        p = _softmax(scores, axis=-1)
        o = torch.einsum("bhqk,bhkd->bhqd", p, v)
        return (lambda x, *d: x.permute(*d))(o, 0, 2, 1, 3).reshape(B, Sq, Hq * Dh) @ wo

    def _B_ref(x, k_cache, v_cache, wq, wo):
        B, Sq, D = x.shape
        Hq, Dh = 32, 128
        Hkv, Sk = k_cache.shape[1], k_cache.shape[2]
        W = min(4096, max(256, Sk // 4))
        rep = Hq // Hkv
        q = (lambda x, *d: x.permute(*d))((x @ wq).reshape(B, Sq, Hq, Dh), 0, 2, 1, 3)
        k = (lambda x, n, axis: torch.repeat_interleave(x, n, dim=axis))(k_cache[:, :, Sk - W:, :], rep, axis=1)
        v = (lambda x, n, axis: torch.repeat_interleave(x, n, dim=axis))(v_cache[:, :, Sk - W:, :], rep, axis=1)
        scores = torch.einsum("bhqd,bhkd->bhqk", q, k) * (Dh ** -0.5)
        p = _softmax(scores, axis=-1)
        o = torch.einsum("bhqk,bhkd->bhqd", p, v)
        return (lambda x, *d: x.permute(*d))(o, 0, 2, 1, 3).reshape(B, Sq, Hq * Dh) @ wo

    x = A_x
    g = _sigmoid((x * gate_w).sum(-1, keepdims=True))
    a = _A_ref(x, A_k_cache, A_v_cache, A_wq, A_wo)
    b = _B_ref(x, B_k_cache, B_v_cache, B_wq, B_wo)
    return g * a + (1 - g) * b
```

```cpp
#include <hip/hip_runtime.h>
#include <hip/hip_bf16.h>
#include <stdint.h>

typedef unsigned short u16;
constexpr int NB = 4, DM = 512, HQ = 32, HKV = 8, SK = 8192, DH = 128, WIN = 2048, NSPLIT = 4;

__device__ __forceinline__ float bf2f(u16 v) { return __uint_as_float(((unsigned)v) << 16); }
__device__ __forceinline__ float rbf(float f) { return __bfloat162float(__float2bfloat16(f)); }

__global__ void k_q(const u16* x, const u16* wqA, const u16* wqB, float* q) {
  __shared__ float xs[NB * DM];
  __shared__ float red[4][NB][64];
  const int br = blockIdx.y;
  const u16* wq = br ? wqB : wqA;
  for (int i = threadIdx.x; i < NB * DM; i += 256) xs[i] = bf2f(x[i]);
  __syncthreads();
  const int col = blockIdx.x * 64 + (threadIdx.x & 63), part = threadIdx.x >> 6;
  float acc[NB] = {0, 0, 0, 0};
  for (int r = part * 128; r < part * 128 + 128; ++r) {
    const float w = bf2f(wq[(size_t)r * 4096 + col]);
    for (int b = 0; b < NB; ++b) acc[b] = fmaf(xs[b * DM + r], w, acc[b]);
  }
  for (int b = 0; b < NB; ++b) red[part][b][threadIdx.x & 63] = acc[b];
  __syncthreads();
  const int b = threadIdx.x >> 6, c = threadIdx.x & 63;
  const float s = red[0][b][c] + red[1][b][c] + red[2][b][c] + red[3][b][c];
  q[((size_t)br * NB + b) * 4096 + blockIdx.x * 64 + c] = rbf(s);
}

__global__ void __launch_bounds__(256) k_attn(const u16* kA, const u16* vA, const u16* kB, const u16* vB, const float* q, float* po, float* pml) {
  __shared__ float qs[4 * DH];
  __shared__ float sc[4][512];
  __shared__ float red[4][256];
  __shared__ float oacc[2][4][DH];
  const int split = blockIdx.x & 3, kvh = (blockIdx.x >> 2) & 7, b = (blockIdx.x >> 5) & 3, br = blockIdx.x >> 7;
  const u16* K = (br ? kB : kA) + ((size_t)(b * HKV + kvh) * SK + (SK - WIN) + split * 512) * DH;
  const u16* V = (br ? vB : vA) + ((size_t)(b * HKV + kvh) * SK + (SK - WIN) + split * 512) * DH;
  const int t = threadIdx.x;
  for (int i = t; i < 4 * DH; i += 256) qs[i] = q[((size_t)br * NB + b) * 4096 + kvh * 512 + i];
  __syncthreads();
_Pragma("unroll 1") for (int key = t; key < 512; key += 256) {
    float a[4] = {0, 0, 0, 0};
_Pragma("unroll 2") for (int d = 0; d < DH; d += 8) {
      const uint4 kv = *(const uint4*)(K + (size_t)key * DH + d);
      const unsigned w[4] = {kv.x, kv.y, kv.z, kv.w};
      for (int j = 0; j < 4; ++j) {
        const float k0 = __uint_as_float(w[j] << 16), k1 = __uint_as_float(w[j] & 0xffff0000u);
        for (int h = 0; h < 4; ++h) a[h] = fmaf(k0, qs[h * DH + d + 2 * j], fmaf(k1, qs[h * DH + d + 2 * j + 1], a[h]));
      }
    }
    for (int h = 0; h < 4; ++h) sc[h][key] = rbf(rbf(a[h]) * 0.08838834764831845f);
  }
  __syncthreads();
  float m[4], l[4];
  for (int h = 0; h < 4; ++h) red[h][t] = fmaxf(sc[h][t], sc[h][t + 256]);
  __syncthreads();
  for (int s = 128; s > 0; s >>= 1) { if (t < s) for (int h = 0; h < 4; ++h) red[h][t] = fmaxf(red[h][t], red[h][t + s]); __syncthreads(); }
  for (int h = 0; h < 4; ++h) m[h] = red[h][0];
  __syncthreads();
  for (int h = 0; h < 4; ++h) {
    const float p0 = __expf(sc[h][t] - m[h]), p1 = __expf(sc[h][t + 256] - m[h]);
    sc[h][t] = p0; sc[h][t + 256] = p1; red[h][t] = p0 + p1;
  }
  __syncthreads();
  for (int s = 128; s > 0; s >>= 1) { if (t < s) for (int h = 0; h < 4; ++h) red[h][t] += red[h][t + s]; __syncthreads(); }
  for (int h = 0; h < 4; ++h) l[h] = red[h][0];
  const int d = t & 127, half = t >> 7;
  float o[4] = {0, 0, 0, 0};
  for (int key = half * 256; key < half * 256 + 256; ++key) {
    const float v = bf2f(V[(size_t)key * DH + d]);
    for (int h = 0; h < 4; ++h) o[h] = fmaf(sc[h][key], v, o[h]);
  }
  for (int h = 0; h < 4; ++h) oacc[half][h][d] = o[h];
  __syncthreads();
  for (int i = t; i < 512; i += 256) po[(size_t)blockIdx.x * 512 + i] = oacc[0][i >> 7][i & 127] + oacc[1][i >> 7][i & 127];
  if (t < 4) { pml[blockIdx.x * 8 + t * 2] = m[t]; pml[blockIdx.x * 8 + t * 2 + 1] = l[t]; }
}

__global__ void k_o(const u16* woA, const u16* woB, const float* po, const float* pml, float* part) {
  __shared__ float os[NB][DH];
  const int h = blockIdx.x & 31, br = blockIdx.x >> 5, kvh = h >> 2, hh = h & 3;
  const u16* wo = (br ? woB : woA) + (size_t)h * DH * DM;
  const int t = threadIdx.x;
  for (int i = t; i < NB * DH; i += 256) {
    const int b = i >> 7, d = i & 127;
    const int blk0 = ((br * NB + b) * HKV + kvh) * 4;
    float M = -INFINITY;
    for (int s = 0; s < 4; ++s) M = fmaxf(M, pml[(blk0 + s) * 8 + hh * 2]);
    float L = 0, o = 0;
    for (int s = 0; s < 4; ++s) {
      const float w = __expf(pml[(blk0 + s) * 8 + hh * 2] - M);
      L += w * pml[(blk0 + s) * 8 + hh * 2 + 1];
      o += w * po[(size_t)(blk0 + s) * 512 + hh * 128 + d];
    }
    os[b][d] = rbf(o / L);
  }
  __syncthreads();
  float acc[NB][2] = {};
  for (int r = 0; r < DH; ++r) {
    const unsigned w = *(const unsigned*)(wo + (size_t)r * DM + 2 * t);
    const float w0 = __uint_as_float(w << 16), w1 = __uint_as_float(w & 0xffff0000u);
    for (int b = 0; b < NB; ++b) { acc[b][0] = fmaf(os[b][r], w0, acc[b][0]); acc[b][1] = fmaf(os[b][r], w1, acc[b][1]); }
  }
  for (int b = 0; b < NB; ++b) { part[((size_t)blockIdx.x * NB + b) * DM + 2 * t] = acc[b][0]; part[((size_t)blockIdx.x * NB + b) * DM + 2 * t + 1] = acc[b][1]; }
}

__global__ void k_fin(const u16* x, const u16* gw, const float* part, __hip_bfloat16* out) {
  __shared__ float red[256];
  const int b = blockIdx.y, t = threadIdx.x;
  float z = 0;
  for (int i = t; i < DM; i += 256) z += rbf(bf2f(x[b * DM + i]) * bf2f(gw[i]));
  red[t] = z; __syncthreads();
  for (int s = 128; s > 0; s >>= 1) { if (t < s) red[t] += red[t + s]; __syncthreads(); }
  z = rbf(red[0]);
  const float g = rbf(1.f / (1.f + __expf(-z)));
  const int c = blockIdx.x * 256 + t;
  float a = 0, bb = 0;
  for (int h = 0; h < 32; ++h) { a += part[((size_t)h * NB + b) * DM + c]; bb += part[((size_t)(32 + h) * NB + b) * DM + c]; }
  out[b * DM + c] = __float2bfloat16(g * a + (1.f - g) * bb);
}

extern "C" void kernel_launch(void* const* d_in, const int* in_sizes, int n_in, void* d_out, int out_size, void* d_ws, size_t ws_size, hipStream_t stream) {
  const u16* x = (const u16*)d_in[0];
  const u16 *kA = (const u16*)d_in[1], *vA = (const u16*)d_in[2], *wqA = (const u16*)d_in[3], *woA = (const u16*)d_in[4];
  const u16 *kB = (const u16*)d_in[6], *vB = (const u16*)d_in[7], *wqB = (const u16*)d_in[8], *woB = (const u16*)d_in[9];
  const u16* gw = (const u16*)d_in[10];
  float* ws = (float*)d_ws;
  float* q = ws; float* po = q + 2 * 4 * 4096; float* pml = po + 256 * 512; float* part = pml + 256 * 8;
  k_q<<<dim3(64, 2), 256, 0, stream>>>(x, wqA, wqB, q);
  k_attn<<<256, 256, 0, stream>>>(kA, vA, kB, vB, q, po, pml);
  k_o<<<64, 256, 0, stream>>>(woA, woB, po, pml, part);
  k_fin<<<dim3(2, 4), 256, 0, stream>>>(x, gw, part, (__hip_bfloat16*)d_out);
}
```

```cpp
#include <hip/hip_runtime.h>
#include <hip/hip_bf16.h>
#include <stdint.h>

typedef unsigned u32;
typedef unsigned long long u64;
typedef u32 u32x4 __attribute__((ext_vector_type(4)));
typedef u32 u32x2 __attribute__((ext_vector_type(2)));
typedef float f32x4 __attribute__((ext_vector_type(4)));
typedef float f32x2 __attribute__((ext_vector_type(2)));
typedef __bf16 bf16x8 __attribute__((ext_vector_type(8)));
typedef __bf16 bf16x2 __attribute__((ext_vector_type(2)));
typedef __amdgpu_buffer_rsrc_t rsrc_t;
#define LAS __attribute__((address_space(3)))

namespace {
constexpr int SK = 8192, WIN = 2048;
constexpr u32 WS_EPOCH = 0, WS_FLAG = 4096, WS_ML = 8192, WS_QP = 65536, WS_OP = WS_QP + 256 * 2048,
              WS_FP = WS_OP + 256 * 2048, WS_END = WS_FP + 256 * 2048;
constexpr u32 L_K = 0, L_Q = 131072, L_ZERO = L_Q + 1280, L_P = L_ZERO + 256, L_MAX = L_P + 4 * 1280, L_SUM = L_MAX + 64,
              L_CTL = L_SUM + 64, L_MRG1 = L_CTL + 64, L_MRG2 = L_MRG1 + 8192, L_END = L_MRG2 + 8192;
constexpr u32 SENT = 0xFFFFFFFFu;
constexpr int AUX_NT = 2, AUX_SC1 = 16;
#define STREAM_AUX AUX_NT
constexpr u32 SPIN_CAP = 1u << 15;
constexpr float SCALE = 0.08838834764831845f, LOG2E = 1.4426950408889634f;

__device__ __forceinline__ float asf(u32 v) { return __builtin_bit_cast(float, v); }
__device__ __forceinline__ u32 asu(float v) { return __builtin_bit_cast(u32, v); }
__device__ __forceinline__ rsrc_t mk(const void* p, u32 bytes) { return __builtin_amdgcn_make_buffer_rsrc((void*)p, (short)0, (int)bytes, 0x00020000); }
__device__ __forceinline__ u32 pk(float a, float b) { const bf16x2 v = __builtin_convertvector(f32x2{a, b}, bf16x2); return __builtin_bit_cast(u32, v); }
__device__ __forceinline__ float rbf(float a) { return asf(pk(a, a) & 0xffff0000u); }
__device__ __forceinline__ float lo2f(u32 v) { return asf(v << 16); }
__device__ __forceinline__ float hi2f(u32 v) { return asf(v & 0xffff0000u); }
template <int CTRL> __device__ __forceinline__ float dpp(float x) {
  return asf((u32)__builtin_amdgcn_mov_dpp((int)asu(x), CTRL, 0xf, 0xf, true));
}
constexpr int XOR1 = 0xB1, XOR2 = 0x4E, ROR4 = 0x124, ROR8 = 0x128;
__device__ __forceinline__ float row_max(float x) { x = fmaxf(x, dpp<XOR1>(x)); x = fmaxf(x, dpp<XOR2>(x)); x = fmaxf(x, dpp<ROR4>(x)); return fmaxf(x, dpp<ROR8>(x)); }
__device__ __forceinline__ float row_sum(float x) { x += dpp<XOR1>(x); x += dpp<XOR2>(x); x += dpp<ROR4>(x); return x + dpp<ROR8>(x); }
__device__ __forceinline__ float xrow_sum(float x) {
  auto s = __builtin_amdgcn_permlane16_swap(asu(x), asu(x), false, false);
  x = asf(s[0]) + asf(s[1]);
  auto t = __builtin_amdgcn_permlane32_swap(asu(x), asu(x), false, false);
  return asf(t[0]) + asf(t[1]);
}
__device__ __forceinline__ f32x4 mfma(u32x4 a, u32x4 b, f32x4 c) {
  return __builtin_amdgcn_mfma_f32_16x16x32_bf16(__builtin_bit_cast(bf16x8, a), __builtin_bit_cast(bf16x8, b), c, 0, 0, 0);
}
template <int J> __device__ __forceinline__ u32x4 bfrag(const u32x4 (&t)[8]) {
  constexpr u32 sel = (J & 1) ? 0x07060302u : 0x05040100u;
  return u32x4{__builtin_amdgcn_perm(t[1][J >> 1], t[0][J >> 1], sel), __builtin_amdgcn_perm(t[3][J >> 1], t[2][J >> 1], sel),
               __builtin_amdgcn_perm(t[5][J >> 1], t[4][J >> 1], sel), __builtin_amdgcn_perm(t[7][J >> 1], t[6][J >> 1], sel)};
}
__device__ __forceinline__ void tile_mma(const u32x4 (&am)[4], const u32x4 (&t)[8], f32x4 (&acc)[2]) {
  acc[0] = mfma(am[0], bfrag<0>(t), acc[0]); acc[1] = mfma(am[0], bfrag<4>(t), acc[1]);
  acc[0] = mfma(am[1], bfrag<1>(t), acc[0]); acc[1] = mfma(am[1], bfrag<5>(t), acc[1]);
  acc[0] = mfma(am[2], bfrag<2>(t), acc[0]); acc[1] = mfma(am[2], bfrag<6>(t), acc[1]);
  acc[0] = mfma(am[3], bfrag<3>(t), acc[0]); acc[1] = mfma(am[3], bfrag<7>(t), acc[1]);
}
__device__ __forceinline__ void mask4(u32x4 a, int lane, u32x4 (&am)[4]) {
#pragma unroll
  for (int t = 0; t < 4; ++t) am[t] = ((lane & 3) == t) ? a : u32x4{0u, 0u, 0u, 0u};
}
__device__ __forceinline__ u32x4 clean(f32x4 v) {
  const u32x4 u = __builtin_bit_cast(u32x4, v);
  return u32x4{min(u.x, SENT - 1), min(u.y, SENT - 1), min(u.z, SENT - 1), min(u.w, SENT - 1)};
}
__device__ __forceinline__ u32 max4(u32x4 v) { return max(max(v.x, v.y), max(v.z, v.w)); }
__device__ __forceinline__ u32x4 lds_r128(u32 addr) { return *(const LAS u32x4*)addr; }
__device__ __forceinline__ void lds_w128(u32 addr, u32x4 v) { *(LAS u32x4*)addr = v; }
#define FENCE() do { asm volatile("" ::: "memory"); __builtin_amdgcn_sched_barrier(0); } while (0)
#define WG_BARRIER() do { __builtin_amdgcn_sched_barrier(0); asm volatile("s_waitcnt lgkmcnt(0)\n\ts_barrier" ::: "memory"); __builtin_amdgcn_sched_barrier(0); } while (0)
#define STAMP(i)
#define STAMP_OUT(n)
}

__global__ void __launch_bounds__(320, 2)
gated_mix_decode_kernel(const char* x, const char* kA, const char* vA, const char* wqA, const char* woA, const char* kB, const char* vB,
                        const char* wqB, const char* woB, const char* gw, char* out, char* ws) {
  extern __shared__ __attribute__((aligned(16))) char lds_[];
  const int lane = threadIdx.x & 63, wave = __builtin_amdgcn_readfirstlane(threadIdx.x >> 6);
  const int g = lane >> 4, c = lane & 15;
  const u32 id = blockIdx.x, r5 = id >> 3;
  const u32 grp = (id & 7) * 2 + (r5 >> 4), gi = r5 & 15, cu = grp * 16 + gi;
  const u32 br = grp >> 3, kvh = grp & 7, hi2 = gi >> 2, lo2 = gi & 3;
  const rsrc_t rs_ws = mk(ws, WS_END);
  STAMP(0);

  if (wave == 4) {
    __builtin_amdgcn_s_setprio(3);
    const rsrc_t rs_x = mk(x, 4096), rs_gw = mk(gw, 1024);
    const rsrc_t rs_wq = mk((br ? wqB : wqA) + (size_t)lo2 * (128 * 8192) + kvh * 1024 + hi2 * 256, 127 * 8192 + 256);
    const u32x2 ep = __builtin_amdgcn_raw_buffer_load_b64(rs_ws, 0, WS_EPOCH, AUX_SC1);
    u32x4 xa[4], w[4][8];
#pragma unroll
    for (int s = 0; s < 4; ++s) xa[s] = __builtin_amdgcn_raw_buffer_load_b128(rs_x, (c >> 2) * 1024 + g * 16 + s * 64, lo2 * 256, 0);
    FENCE();
#pragma unroll
    for (int s = 0; s < 4; ++s)
#pragma unroll
      for (int j = 0; j < 8; ++j) w[s][j] = __builtin_amdgcn_raw_buffer_load_b128(rs_wq, g * (8 * 8192) + c * 16, (32 * s + j) * 8192, STREAM_AUX);
    FENCE();
    if (lane < 16) lds_w128(L_ZERO + lane * 16, u32x4{0u, 0u, 0u, 0u});
    if (lane == 0) *(volatile LAS u32*)(L_CTL) = 0u;
    WG_BARRIER();
    STAMP(1);
    const u32x4 gx = __builtin_amdgcn_raw_buffer_load_b128(rs_x, lane * 16, ((cu >> 4) & 3) * 1024, 0);
    const u32x4 gv = __builtin_amdgcn_raw_buffer_load_b128(rs_gw, lane * 16, 0, 0);
    {
      const u32x4 s4 = {SENT, SENT, SENT, SENT};
      __builtin_amdgcn_raw_buffer_store_b128(s4, rs_ws, lane * 16, WS_OP + cu * 2048, AUX_SC1);
      __builtin_amdgcn_raw_buffer_store_b128(s4, rs_ws, lane * 16 + 1024, WS_OP + cu * 2048, AUX_SC1);
      __builtin_amdgcn_raw_buffer_store_b128(s4, rs_ws, lane * 16, WS_FP + cu * 2048, AUX_SC1);
      __builtin_amdgcn_raw_buffer_store_b128(s4, rs_ws, lane * 16 + 1024, WS_FP + cu * 2048, AUX_SC1);
      if (lane < 2) __builtin_amdgcn_raw_buffer_store_b128(s4, rs_ws, lane * 16, WS_ML + cu * 32, AUX_SC1);
    }
    FENCE();
    f32x4 acc[2] = {{0.f, 0.f, 0.f, 0.f}, {0.f, 0.f, 0.f, 0.f}};
#pragma unroll
    for (int s = 0; s < 4; ++s) { u32x4 am[4]; mask4(xa[s], lane, am); tile_mma(am, w[s], acc); }
    __builtin_amdgcn_raw_buffer_store_b128(__builtin_bit_cast(u32x4, acc[0]), rs_ws, lane * 16, WS_QP + cu * 2048, AUX_SC1);
    __builtin_amdgcn_raw_buffer_store_b128(__builtin_bit_cast(u32x4, acc[1]), rs_ws, lane * 16 + 1024, WS_QP + cu * 2048, AUX_SC1);
    STAMP(2);
    const u64 tag64 = (((u64)ep.y << 32) | ep.x) + 1;
    const u32x2 tag = {(u32)tag64, (u32)(tag64 >> 32)};
    asm volatile("s_waitcnt vmcnt(0)" ::: "memory");
    if (lane == 0) __builtin_amdgcn_raw_buffer_store_b64(tag, rs_ws, 0, WS_FLAG + cu * 8, AUX_SC1);
    STAMP(3);
    {
      float z = 0.f;
#pragma unroll
      for (int i = 0; i < 4; ++i) z += rbf(lo2f(gx[i]) * lo2f(gv[i])) + rbf(hi2f(gx[i]) * hi2f(gv[i]));
      z = rbf(xrow_sum(row_sum(z)));
      const float gt = rbf(1.f / (1.f + __expf(-z)));
      if (lane == 0) *(volatile LAS float*)(L_CTL + 4) = gt;
    }
    for (u32 spin = 0; spin < SPIN_CAP; ++spin) {
      asm volatile("" ::: "memory");
      const u32x4 f0 = __builtin_amdgcn_raw_buffer_load_b128(rs_ws, lane * 16, WS_FLAG, AUX_SC1);
      const u32x4 f1 = __builtin_amdgcn_raw_buffer_load_b128(rs_ws, lane * 16 + 1024, WS_FLAG, AUX_SC1);
      const bool ok = f0.x == tag.x && f0.y == tag.y && f0.z == tag.x && f0.w == tag.y && f1.x == tag.x && f1.y == tag.y && f1.z == tag.x && f1.w == tag.y;
      if (__all(ok)) break;
      __builtin_amdgcn_s_sleep(4);
    }
    FENCE();
    STAMP(4);
    {
      u32x4 p[4][2];
#pragma unroll
      for (int kq = 0; kq < 4; ++kq)
#pragma unroll
        for (int a = 0; a < 2; ++a)
          p[kq][a] = __builtin_amdgcn_raw_buffer_load_b128(rs_ws, g * 8192 + c * 16 + a * 1024, WS_QP + (grp * 16 + kq) * 2048 + hi2 * 256, AUX_SC1);
      f32x4 q[2];
#pragma unroll
      for (int a = 0; a < 2; ++a)
        q[a] = (__builtin_bit_cast(f32x4, p[0][a]) + __builtin_bit_cast(f32x4, p[1][a])) + (__builtin_bit_cast(f32x4, p[2][a]) + __builtin_bit_cast(f32x4, p[3][a]));
      lds_w128(L_Q + g * 320 + c * 16, u32x4{pk(q[0].x, q[0].y), pk(q[0].z, q[0].w), pk(q[1].x, q[1].y), pk(q[1].z, q[1].w)});
    }
    asm volatile("s_waitcnt lgkmcnt(0)" ::: "memory");
    if (lane == 0) *(volatile LAS u32*)(L_CTL) = 1u;
    if (id == 0 && lane == 0) __builtin_amdgcn_raw_buffer_store_b64(tag, rs_ws, 0, WS_EPOCH, AUX_SC1);
    STAMP(5);
    STAMP_OUT(6);
    return;
  }

  const size_t kvoff = ((size_t)(hi2 * 8 + kvh) * SK + (SK - WIN) + lo2 * 512) * 256 + wave * 1024;
  const rsrc_t rs_k = mk((br ? kB : kA) + kvoff, 131072 - wave * 1024), rs_v = mk((br ? vB : vA) + kvoff, 131072 - wave * 1024);
  const rsrc_t rs_wo = mk((br ? woB : woA) + (size_t)((kvh * 4 + hi2) * 128 + wave * 32) * 1024 + lo2 * 256, 31 * 1024 + 256);
  const u32 rot = r5 & 31;
  const u32 kw = L_K + wave * 32768;
  WG_BARRIER();
  STAMP(1);
  {
    const u32 voff0 = g * 256 + ((c ^ g) << 4);
#pragma unroll
    for (int p = 0; p < 32; ++p) {
      const u32 so = ((p + rot) & 31) << 12, m0v = kw + p * 1024, vo = voff0 ^ ((p >> 3) << 6);
      if (STREAM_AUX) asm volatile("s_mov_b32 m0, %0\n\ts_nop 0\n\tbuffer_load_dwordx4 %1, %2, %3 offen nt lds" :: "s"(m0v), "v"(vo), "s"(rs_k), "s"(so) : "memory");
      else asm volatile("s_mov_b32 m0, %0\n\ts_nop 0\n\tbuffer_load_dwordx4 %1, %2, %3 offen lds" :: "s"(m0v), "v"(vo), "s"(rs_k), "s"(so) : "memory");
    }
  }
  FENCE();
  STAMP(2);
  u32x4 v[4][8];
#define LOAD_V(u, j) v[u][j] = __builtin_amdgcn_raw_buffer_load_b128(rs_v, lane * 16, ((8 * (u) + (j) + rot) & 31) << 12, STREAM_AUX)
#pragma unroll
  for (int j = 0; j < 8; ++j) LOAD_V(0, j);
#pragma unroll
  for (int j = 0; j < 8; ++j) LOAD_V(1, j);
  FENCE();
  STAMP(3);
  for (u32 spin = 0; spin < (SPIN_CAP << 6); ++spin) {
    if (*(volatile LAS u32*)(L_CTL)) break;
    __builtin_amdgcn_s_sleep(2);
  }
  asm volatile("s_waitcnt vmcnt(16)" ::: "memory");
  FENCE();
  STAMP(4);
  f32x4 sc[2] = {{0.f, 0.f, 0.f, 0.f}, {0.f, 0.f, 0.f, 0.f}};
  {
    const u32 ka = kw + (c >> 2) * 8192 + (c & 3) * 256 + ((g ^ c) << 4);
#pragma unroll
    for (int t = 0; t < 4; ++t) {
      const u32 qa = ((c & 3) == t) ? L_Q + (c >> 2) * 320 + g * 16 : L_ZERO;
      u32x4 qf[4], kf[2][4];
#pragma unroll
      for (int s = 0; s < 4; ++s) qf[s] = lds_r128(qa + s * 64);
#pragma unroll
      for (int a = 0; a < 2; ++a)
#pragma unroll
        for (int s = 0; s < 4; ++s) kf[a][s] = lds_r128((ka ^ (s << 6)) + (4 * a + t) * 1024);
      LOAD_V(2, 2 * t); LOAD_V(2, 2 * t + 1);
#pragma unroll
      for (int s = 0; s < 4; ++s) { sc[0] = mfma(qf[s], kf[0][s], sc[0]); sc[1] = mfma(qf[s], kf[1][s], sc[1]); }
      FENCE();
    }
  }
#pragma unroll
  for (int j = 0; j < 8; ++j) LOAD_V(3, j);
  FENCE();
  u32x4 wo[8];
#pragma unroll
  for (int j = 0; j < 8; ++j) wo[j] = __builtin_amdgcn_raw_buffer_load_b128(rs_wo, g * (8 * 1024) + c * 16, j * 1024, STREAM_AUX);
  FENCE();
  STAMP(5);
  float sv[8];
#pragma unroll
  for (int i = 0; i < 8; ++i) sv[i] = rbf(rbf(sc[i >> 2][i & 3]) * SCALE);
  float m = fmaxf(fmaxf(fmaxf(sv[0], sv[1]), fmaxf(sv[2], sv[3])), fmaxf(fmaxf(sv[4], sv[5]), fmaxf(sv[6], sv[7])));
  m = row_max(m);
  if (c == 0) *(LAS float*)(L_MAX + g * 16 + wave * 4) = m;
  WG_BARRIER();
  {
    const f32x4 mm = __builtin_bit_cast(f32x4, lds_r128(L_MAX + g * 16));
    m = fmaxf(fmaxf(mm.x, mm.y), fmaxf(mm.z, mm.w));
  }
  float l = 0.f;
#pragma unroll
  for (int i = 0; i < 8; ++i) { sv[i] = __builtin_amdgcn_exp2f((sv[i] - m) * LOG2E); l += sv[i]; }
  l = row_sum(l);
  if (c == 0) *(LAS float*)(L_SUM + g * 16 + wave * 4) = l;
  const u32 pw = L_P + wave * 1280;
  lds_w128(pw + g * 320 + c * 16, u32x4{pk(sv[0], sv[1]), pk(sv[2], sv[3]), pk(sv[4], sv[5]), pk(sv[6], sv[7])});
  FENCE();
  STAMP(6);
  f32x4 acc[2] = {{0.f, 0.f, 0.f, 0.f}, {0.f, 0.f, 0.f, 0.f}};
#pragma unroll
  for (int u = 0; u < 4; ++u) {
    u32x4 am[4];
#pragma unroll
    for (int t = 0; t < 4; ++t) am[t] = lds_r128((((c & 3) == t) ? pw + (c >> 2) * 320 + g * 16 : L_ZERO) + u * 64);
    tile_mma(am, v[u], acc);
    FENCE();
  }
  STAMP(7);
  lds_w128(L_MRG1 + wave * 2048 + lane * 16, __builtin_bit_cast(u32x4, acc[0]));
  lds_w128(L_MRG1 + wave * 2048 + 1024 + lane * 16, __builtin_bit_cast(u32x4, acc[1]));
  WG_BARRIER();
  if (wave < 2) {
    const u32 a = L_MRG1 + wave * 1024 + lane * 16;
    const f32x4 o = (__builtin_bit_cast(f32x4, lds_r128(a)) + __builtin_bit_cast(f32x4, lds_r128(a + 2048))) +
                    (__builtin_bit_cast(f32x4, lds_r128(a + 4096)) + __builtin_bit_cast(f32x4, lds_r128(a + 6144)));
    __builtin_amdgcn_raw_buffer_store_b128(clean(o), rs_ws, lane * 16, WS_OP + cu * 2048 + wave * 1024, AUX_SC1);
  } else if (wave == 2) {
    const f32x4 ls = __builtin_bit_cast(f32x4, lds_r128(L_SUM + g * 16));
    const float lt = (ls.x + ls.y) + (ls.z + ls.w);
    if (c == 0) __builtin_amdgcn_raw_buffer_store_b64(u32x2{min(asu(m), SENT - 1), min(asu(lt), SENT - 1)}, rs_ws, g * 8, WS_ML + cu * 32, AUX_SC1);
  }
  FENCE();
  STAMP(8);
  u32x4 bf[8] = {bfrag<0>(wo), bfrag<1>(wo), bfrag<2>(wo), bfrag<3>(wo), bfrag<4>(wo), bfrag<5>(wo), bfrag<6>(wo), bfrag<7>(wo)};
#pragma unroll
  for (int j = 0; j < 8; ++j) asm volatile("" : "+v"(bf[j]));
  FENCE();
  STAMP(9);
  u32x4 o0, o1; u32x2 ml;
  for (u32 spin = 0; spin < SPIN_CAP; ++spin) {
    asm volatile("" ::: "memory");
    o0 = __builtin_amdgcn_raw_buffer_load_b128(rs_ws, c * 2048 + g * 16, WS_OP + grp * 32768 + (hi2 * 16 + wave * 4) * 16, AUX_SC1);
    o1 = __builtin_amdgcn_raw_buffer_load_b128(rs_ws, c * 2048 + g * 16 + 1024, WS_OP + grp * 32768 + (hi2 * 16 + wave * 4) * 16, AUX_SC1);
    ml = __builtin_amdgcn_raw_buffer_load_b64(rs_ws, c * 32, WS_ML + grp * 512 + hi2 * 8, AUX_SC1);
    if (__all(max(max(max4(o0), max4(o1)), max(ml.x, ml.y)) != SENT)) break;
  }
  FENCE();
  STAMP(10);
  f32x4 oa[2] = {{0.f, 0.f, 0.f, 0.f}, {0.f, 0.f, 0.f, 0.f}};
  {
    const float ms = asf(ml.x), ls = asf(ml.y);
    float M = fmaxf(ms, dpp<XOR1>(ms)); M = fmaxf(M, dpp<XOR2>(M));
    const float e = __builtin_amdgcn_exp2f((ms - M) * LOG2E);
    float L = e * ls; L += dpp<XOR1>(L); L += dpp<XOR2>(L);
    const float f = e / L;
    float y[8];
#pragma unroll
    for (int j = 0; j < 8; ++j) { float t = f * asf(j < 4 ? o0[j & 3] : o1[j & 3]); t += dpp<XOR1>(t); y[j] = t + dpp<XOR2>(t); }
    u32x4 am[4];
    mask4(u32x4{pk(y[0], y[1]), pk(y[2], y[3]), pk(y[4], y[5]), pk(y[6], y[7])}, lane, am);
#pragma unroll
    for (int t = 0; t < 4; ++t) { oa[0] = mfma(am[t], bf[t], oa[0]); oa[1] = mfma(am[t], bf[4 + t], oa[1]); }
  }
  lds_w128(L_MRG2 + wave * 2048 + lane * 16, __builtin_bit_cast(u32x4, oa[0]));
  lds_w128(L_MRG2 + wave * 2048 + 1024 + lane * 16, __builtin_bit_cast(u32x4, oa[1]));
  WG_BARRIER();
  STAMP(11);
  if (wave < 2) {
    const u32 ln = wave * 32 + (lane >> 1), a = L_MRG2 + (lane & 1) * 1024 + ln * 16;
    const f32x4 o = (__builtin_bit_cast(f32x4, lds_r128(a)) + __builtin_bit_cast(f32x4, lds_r128(a + 2048))) +
                    (__builtin_bit_cast(f32x4, lds_r128(a + 4096)) + __builtin_bit_cast(f32x4, lds_r128(a + 6144)));
    __builtin_amdgcn_raw_buffer_store_b128(clean(o), rs_ws, ln * 2048 + (lane & 1) * 16, WS_FP + lo2 * (64 * 2048) + (grp * 4 + hi2) * 32, AUX_SC1);
  } else if (wave == 3) {
    u32x4 pa, pb;
    for (u32 spin = 0; spin < SPIN_CAP; ++spin) {
      asm volatile("" ::: "memory");
      pa = __builtin_amdgcn_raw_buffer_load_b128(rs_ws, lane * 16, WS_FP + cu * 2048, AUX_SC1);
      pb = __builtin_amdgcn_raw_buffer_load_b128(rs_ws, lane * 16 + 1024, WS_FP + cu * 2048, AUX_SC1);
      if (__all(max(max4(pa), max4(pb)) != SENT)) break;
    }
    FENCE();
    STAMP(12);
    const float gt = *(volatile LAS float*)(L_CTL + 4), gn = rbf(1.f - gt);
    float y[4];
#pragma unroll
    for (int i = 0; i < 4; ++i) {
      float a = asf(pa[i]), b = asf(pb[i]);
      a += dpp<XOR2>(a); a += dpp<ROR4>(a); a += dpp<ROR8>(a); a = xrow_sum(a);
      b += dpp<XOR2>(b); b += dpp<ROR4>(b); b += dpp<ROR8>(b); b = xrow_sum(b);
      y[i] = rbf(gt * rbf(a)) + rbf(gn * rbf(b));
    }
    const rsrc_t rs_out = mk(out, 4096);
    if (lane < 2) __builtin_amdgcn_raw_buffer_store_b64(u32x2{pk(y[0], y[1]), pk(y[2], y[3])}, rs_out, lane * 8, ((cu >> 4) & 3) * 1024 + (cu >> 6) * 256 + (cu & 15) * 16, 0);
  }
  STAMP(13);
  STAMP_OUT(14);
}

extern "C" void kernel_launch(void* const* d_in, const int* in_sizes, int n_in, void* d_out, int out_size, void* d_ws, size_t ws_size,
                              hipStream_t stream) {
  (void)in_sizes; (void)n_in; (void)out_size; (void)ws_size;
  (void)hipFuncSetAttribute((const void*)gated_mix_decode_kernel, hipFuncAttributeMaxDynamicSharedMemorySize, (int)L_END);
  hipLaunchKernelGGL(gated_mix_decode_kernel, dim3(256), dim3(320), L_END, stream, (const char*)d_in[0], (const char*)d_in[1], (const char*)d_in[2],
                     (const char*)d_in[3], (const char*)d_in[4], (const char*)d_in[6], (const char*)d_in[7], (const char*)d_in[8],
                     (const char*)d_in[9], (const char*)d_in[10], (char*)d_out, (char*)d_ws);
}
```

```cpp
#define PROBE 0
#define AMP 4
#define PBLK 0
#include <hip/hip_runtime.h>
#include <hip/hip_bf16.h>
#include <stdint.h>

typedef unsigned u32;
typedef unsigned long long u64;
typedef u32 u32x4 __attribute__((ext_vector_type(4)));
typedef u32 u32x2 __attribute__((ext_vector_type(2)));
typedef float f32x4 __attribute__((ext_vector_type(4)));
typedef float f32x2 __attribute__((ext_vector_type(2)));
typedef __bf16 bf16x8 __attribute__((ext_vector_type(8)));
typedef __bf16 bf16x2 __attribute__((ext_vector_type(2)));
typedef __amdgpu_buffer_rsrc_t rsrc_t;
#define LAS __attribute__((address_space(3)))

namespace {
constexpr int SK = 8192, WIN = 2048;
constexpr u32 WS_EPOCH = 0, WS_FLAG = 4096, WS_ML = 8192, WS_QP = 65536, WS_OP = WS_QP + 256 * 2048,
              WS_FP = WS_OP + 256 * 2048, WS_END = WS_FP + 256 * 2048;
constexpr u32 L_K = 0, L_Q = 131072, L_ZERO = L_Q + 1280, L_P = L_ZERO + 256, L_MAX = L_P + 4 * 1280, L_SUM = L_MAX + 64,
              L_CTL = L_SUM + 64, L_MRG1 = L_CTL + 64, L_MRG2 = L_MRG1 + 8192, L_END = L_MRG2 + 8192;
constexpr u32 SENT = 0xFFFFFFFFu;
constexpr int AUX_NT = 2, AUX_SC1 = 16;
#define STREAM_AUX AUX_NT
constexpr u32 SPIN_CAP = 1u << 15;
constexpr float SCALE = 0.08838834764831845f, LOG2E = 1.4426950408889634f;

__device__ __forceinline__ float asf(u32 v) { return __builtin_bit_cast(float, v); }
__device__ __forceinline__ u32 asu(float v) { return __builtin_bit_cast(u32, v); }
__device__ __forceinline__ rsrc_t mk(const void* p, u32 bytes) { return __builtin_amdgcn_make_buffer_rsrc((void*)p, (short)0, (int)bytes, 0x00020000); }
__device__ __forceinline__ u32 pk(float a, float b) { const bf16x2 v = __builtin_convertvector(f32x2{a, b}, bf16x2); return __builtin_bit_cast(u32, v); }
__device__ __forceinline__ float rbf(float a) { return asf(pk(a, a) & 0xffff0000u); }
__device__ __forceinline__ float lo2f(u32 v) { return asf(v << 16); }
__device__ __forceinline__ float hi2f(u32 v) { return asf(v & 0xffff0000u); }
template <int CTRL> __device__ __forceinline__ float dpp(float x) {
  return asf((u32)__builtin_amdgcn_mov_dpp((int)asu(x), CTRL, 0xf, 0xf, true));
}
constexpr int XOR1 = 0xB1, XOR2 = 0x4E, ROR4 = 0x124, ROR8 = 0x128;
__device__ __forceinline__ float row_max(float x) { x = fmaxf(x, dpp<XOR1>(x)); x = fmaxf(x, dpp<XOR2>(x)); x = fmaxf(x, dpp<ROR4>(x)); return fmaxf(x, dpp<ROR8>(x)); }
__device__ __forceinline__ float row_sum(float x) { x += dpp<XOR1>(x); x += dpp<XOR2>(x); x += dpp<ROR4>(x); return x + dpp<ROR8>(x); }
__device__ __forceinline__ float xrow_sum(float x) {
  auto s = __builtin_amdgcn_permlane16_swap(asu(x), asu(x), false, false);
  x = asf(s[0]) + asf(s[1]);
  auto t = __builtin_amdgcn_permlane32_swap(asu(x), asu(x), false, false);
  return asf(t[0]) + asf(t[1]);
}
__device__ __forceinline__ f32x4 mfma(u32x4 a, u32x4 b, f32x4 c) {
  return __builtin_amdgcn_mfma_f32_16x16x32_bf16(__builtin_bit_cast(bf16x8, a), __builtin_bit_cast(bf16x8, b), c, 0, 0, 0);
}
template <int J> __device__ __forceinline__ u32x4 bfrag(const u32x4 (&t)[8]) {
  constexpr u32 sel = (J & 1) ? 0x07060302u : 0x05040100u;
  return u32x4{__builtin_amdgcn_perm(t[1][J >> 1], t[0][J >> 1], sel), __builtin_amdgcn_perm(t[3][J >> 1], t[2][J >> 1], sel),
               __builtin_amdgcn_perm(t[5][J >> 1], t[4][J >> 1], sel), __builtin_amdgcn_perm(t[7][J >> 1], t[6][J >> 1], sel)};
}
__device__ __forceinline__ void tile_mma(const u32x4 (&am)[4], const u32x4 (&t)[8], f32x4 (&acc)[2]) {
  acc[0] = mfma(am[0], bfrag<0>(t), acc[0]); acc[1] = mfma(am[0], bfrag<4>(t), acc[1]);
  acc[0] = mfma(am[1], bfrag<1>(t), acc[0]); acc[1] = mfma(am[1], bfrag<5>(t), acc[1]);
  acc[0] = mfma(am[2], bfrag<2>(t), acc[0]); acc[1] = mfma(am[2], bfrag<6>(t), acc[1]);
  acc[0] = mfma(am[3], bfrag<3>(t), acc[0]); acc[1] = mfma(am[3], bfrag<7>(t), acc[1]);
}
__device__ __forceinline__ void mask4(u32x4 a, int lane, u32x4 (&am)[4]) {
#pragma unroll
  for (int t = 0; t < 4; ++t) am[t] = ((lane & 3) == t) ? a : u32x4{0u, 0u, 0u, 0u};
}
__device__ __forceinline__ u32x4 clean(f32x4 v) {
  const u32x4 u = __builtin_bit_cast(u32x4, v);
  return u32x4{min(u.x, SENT - 1), min(u.y, SENT - 1), min(u.z, SENT - 1), min(u.w, SENT - 1)};
}
__device__ __forceinline__ u32 max4(u32x4 v) { return max(max(v.x, v.y), max(v.z, v.w)); }
__device__ __forceinline__ u32x4 lds_r128(u32 addr) { return *(const LAS u32x4*)addr; }
__device__ __forceinline__ void lds_w128(u32 addr, u32x4 v) { *(LAS u32x4*)addr = v; }
#define FENCE() do { asm volatile("" ::: "memory"); __builtin_amdgcn_sched_barrier(0); } while (0)
#define WG_BARRIER() do { __builtin_amdgcn_sched_barrier(0); asm volatile("s_waitcnt lgkmcnt(0)\n\ts_barrier" ::: "memory"); __builtin_amdgcn_sched_barrier(0); } while (0)
#define STAMP(i) do { __builtin_amdgcn_sched_barrier(0); asm volatile("s_memrealtime %0\n\ts_waitcnt lgkmcnt(0)" : "=s"(ts[i]) :: "memory"); __builtin_amdgcn_sched_barrier(0); } while (0)
#define STAMP_OUT(n)
}

__global__ void __launch_bounds__(320, 2)
gated_mix_decode_kernel(const char* x, const char* kA, const char* vA, const char* wqA, const char* woA, const char* kB, const char* vB,
                        const char* wqB, const char* woB, const char* gw, char* out, char* ws) {
  extern __shared__ __attribute__((aligned(16))) char lds_[];
  const int lane = threadIdx.x & 63, wave = __builtin_amdgcn_readfirstlane(threadIdx.x >> 6);
  const int g = lane >> 4, c = lane & 15;
  const u32 id = blockIdx.x, r5 = id >> 3;
  const u32 grp = (id & 7) * 2 + (r5 >> 4), gi = r5 & 15, cu = grp * 16 + gi;
  const u32 br = grp >> 3, kvh = grp & 7, hi2 = gi >> 2, lo2 = gi & 3;
  const rsrc_t rs_ws = mk(ws, WS_END);
  u64 ts[16] = {};
  const u64 c0 = __builtin_amdgcn_s_memtime();
  STAMP(0);

  if (wave == 4) {
    __builtin_amdgcn_s_setprio(3);
    const rsrc_t rs_x = mk(x, 4096), rs_gw = mk(gw, 1024);
    const rsrc_t rs_wq = mk((br ? wqB : wqA) + (size_t)lo2 * (128 * 8192) + kvh * 1024 + hi2 * 256, 127 * 8192 + 256);
    const u32x2 ep = __builtin_amdgcn_raw_buffer_load_b64(rs_ws, 0, WS_EPOCH, AUX_SC1);
    u32x4 xa[4], w[4][8];
#pragma unroll
    for (int s = 0; s < 4; ++s) xa[s] = __builtin_amdgcn_raw_buffer_load_b128(rs_x, (c >> 2) * 1024 + g * 16 + s * 64, lo2 * 256, 0);
    FENCE();
#pragma unroll
    for (int s = 0; s < 4; ++s)
#pragma unroll
      for (int j = 0; j < 8; ++j) w[s][j] = __builtin_amdgcn_raw_buffer_load_b128(rs_wq, g * (8 * 8192) + c * 16, (32 * s + j) * 8192, STREAM_AUX);
    FENCE();
    if (lane < 16) lds_w128(L_ZERO + lane * 16, u32x4{0u, 0u, 0u, 0u});
    if (lane == 0) *(volatile LAS u32*)(L_CTL) = 0u;
    WG_BARRIER();
    STAMP(1);
    const u32x4 gx = __builtin_amdgcn_raw_buffer_load_b128(rs_x, lane * 16, ((cu >> 4) & 3) * 1024, 0);
    const u32x4 gv = __builtin_amdgcn_raw_buffer_load_b128(rs_gw, lane * 16, 0, 0);
    {
      const u32x4 s4 = {SENT, SENT, SENT, SENT};
      __builtin_amdgcn_raw_buffer_store_b128(s4, rs_ws, lane * 16, WS_OP + cu * 2048, AUX_SC1);
      __builtin_amdgcn_raw_buffer_store_b128(s4, rs_ws, lane * 16 + 1024, WS_OP + cu * 2048, AUX_SC1);
      __builtin_amdgcn_raw_buffer_store_b128(s4, rs_ws, lane * 16, WS_FP + cu * 2048, AUX_SC1);
      __builtin_amdgcn_raw_buffer_store_b128(s4, rs_ws, lane * 16 + 1024, WS_FP + cu * 2048, AUX_SC1);
      if (lane < 2) __builtin_amdgcn_raw_buffer_store_b128(s4, rs_ws, lane * 16, WS_ML + cu * 32, AUX_SC1);
    }
    FENCE();
    f32x4 acc[2] = {{0.f, 0.f, 0.f, 0.f}, {0.f, 0.f, 0.f, 0.f}};
#pragma unroll
    for (int s = 0; s < 4; ++s) { u32x4 am[4]; mask4(xa[s], lane, am); tile_mma(am, w[s], acc); }
    __builtin_amdgcn_raw_buffer_store_b128(__builtin_bit_cast(u32x4, acc[0]), rs_ws, lane * 16, WS_QP + cu * 2048, AUX_SC1);
    __builtin_amdgcn_raw_buffer_store_b128(__builtin_bit_cast(u32x4, acc[1]), rs_ws, lane * 16 + 1024, WS_QP + cu * 2048, AUX_SC1);
    STAMP(2);
    const u64 tag64 = (((u64)ep.y << 32) | ep.x) + 1;
    const u32x2 tag = {(u32)tag64, (u32)(tag64 >> 32)};
    asm volatile("s_waitcnt vmcnt(0)" ::: "memory");
    if (lane == 0) __builtin_amdgcn_raw_buffer_store_b64(tag, rs_ws, 0, WS_FLAG + cu * 8, AUX_SC1);
    STAMP(3);
    {
      float z = 0.f;
#pragma unroll
      for (int i = 0; i < 4; ++i) z += rbf(lo2f(gx[i]) * lo2f(gv[i])) + rbf(hi2f(gx[i]) * hi2f(gv[i]));
      z = rbf(xrow_sum(row_sum(z)));
      const float gt = rbf(1.f / (1.f + __expf(-z)));
      if (lane == 0) *(volatile LAS float*)(L_CTL + 4) = gt;
    }
    for (u32 spin = 0; spin < SPIN_CAP; ++spin) {
      asm volatile("" ::: "memory");
      const u32x4 f0 = __builtin_amdgcn_raw_buffer_load_b128(rs_ws, lane * 16, WS_FLAG, AUX_SC1);
      const u32x4 f1 = __builtin_amdgcn_raw_buffer_load_b128(rs_ws, lane * 16 + 1024, WS_FLAG, AUX_SC1);
      const bool ok = f0.x == tag.x && f0.y == tag.y && f0.z == tag.x && f0.w == tag.y && f1.x == tag.x && f1.y == tag.y && f1.z == tag.x && f1.w == tag.y;
      if (__all(ok)) break;
      __builtin_amdgcn_s_sleep(4);
    }
    FENCE();
    STAMP(4);
    {
      u32x4 p[4][2];
#pragma unroll
      for (int kq = 0; kq < 4; ++kq)
#pragma unroll
        for (int a = 0; a < 2; ++a)
          p[kq][a] = __builtin_amdgcn_raw_buffer_load_b128(rs_ws, g * 8192 + c * 16 + a * 1024, WS_QP + (grp * 16 + kq) * 2048 + hi2 * 256, AUX_SC1);
      f32x4 q[2];
#pragma unroll
      for (int a = 0; a < 2; ++a)
        q[a] = (__builtin_bit_cast(f32x4, p[0][a]) + __builtin_bit_cast(f32x4, p[1][a])) + (__builtin_bit_cast(f32x4, p[2][a]) + __builtin_bit_cast(f32x4, p[3][a]));
      lds_w128(L_Q + g * 320 + c * 16, u32x4{pk(q[0].x, q[0].y), pk(q[0].z, q[0].w), pk(q[1].x, q[1].y), pk(q[1].z, q[1].w)});
    }
    if (lane == 0) for (int i = 0; i < 5; ++i) *(volatile LAS u32*)(L_CTL + 16 + 4 * i) = (u32)(ts[i] - ts[0]);
    asm volatile("s_waitcnt lgkmcnt(0)" ::: "memory");
    if (lane == 0) *(volatile LAS u32*)(L_CTL) = 1u;
    if (id == 0 && lane == 0) __builtin_amdgcn_raw_buffer_store_b64(tag, rs_ws, 0, WS_EPOCH, AUX_SC1);
    STAMP(5);
    STAMP_OUT(6);
    return;
  }

  const size_t kvoff = ((size_t)(hi2 * 8 + kvh) * SK + (SK - WIN) + lo2 * 512) * 256 + wave * 1024;
  const rsrc_t rs_k = mk((br ? kB : kA) + kvoff, 131072 - wave * 1024), rs_v = mk((br ? vB : vA) + kvoff, 131072 - wave * 1024);
  const rsrc_t rs_wo = mk((br ? woB : woA) + (size_t)((kvh * 4 + hi2) * 128 + wave * 32) * 1024 + lo2 * 256, 31 * 1024 + 256);
  const u32 rot = r5 & 31;
  const u32 kw = L_K + wave * 32768;
  WG_BARRIER();
  STAMP(1);
  {
    const u32 voff0 = g * 256 + ((c ^ g) << 4);
#pragma unroll
    for (int p = 0; p < 32; ++p) {
      const u32 so = ((p + rot) & 31) << 12, m0v = kw + p * 1024, vo = voff0 ^ ((p >> 3) << 6);
      if (STREAM_AUX) asm volatile("s_mov_b32 m0, %0\n\ts_nop 0\n\tbuffer_load_dwordx4 %1, %2, %3 offen nt lds" :: "s"(m0v), "v"(vo), "s"(rs_k), "s"(so) : "memory");
      else asm volatile("s_mov_b32 m0, %0\n\ts_nop 0\n\tbuffer_load_dwordx4 %1, %2, %3 offen lds" :: "s"(m0v), "v"(vo), "s"(rs_k), "s"(so) : "memory");
    }
  }
  FENCE();
  STAMP(2);
  u32x4 v[4][8];
#define LOAD_V(u, j) v[u][j] = __builtin_amdgcn_raw_buffer_load_b128(rs_v, lane * 16, ((8 * (u) + (j) + rot) & 31) << 12, STREAM_AUX)
#pragma unroll
  for (int j = 0; j < 8; ++j) LOAD_V(0, j);
#pragma unroll
  for (int j = 0; j < 8; ++j) LOAD_V(1, j);
  FENCE();
  STAMP(3);
  for (u32 spin = 0; spin < (SPIN_CAP << 6); ++spin) {
    if (*(volatile LAS u32*)(L_CTL)) break;
    __builtin_amdgcn_s_sleep(2);
  }
  asm volatile("s_waitcnt vmcnt(16)" ::: "memory");
  FENCE();
  STAMP(4);
  f32x4 sc[2] = {{0.f, 0.f, 0.f, 0.f}, {0.f, 0.f, 0.f, 0.f}};
  {
    const u32 ka = kw + (c >> 2) * 8192 + (c & 3) * 256 + ((g ^ c) << 4);
#pragma unroll
    for (int t = 0; t < 4; ++t) {
      const u32 qa = ((c & 3) == t) ? L_Q + (c >> 2) * 320 + g * 16 : L_ZERO;
      u32x4 qf[4], kf[2][4];
#pragma unroll
      for (int s = 0; s < 4; ++s) qf[s] = lds_r128(qa + s * 64);
#pragma unroll
      for (int a = 0; a < 2; ++a)
#pragma unroll
        for (int s = 0; s < 4; ++s) kf[a][s] = lds_r128((ka ^ (s << 6)) + (4 * a + t) * 1024);
      LOAD_V(2, 2 * t); LOAD_V(2, 2 * t + 1);
#pragma unroll
      for (int s = 0; s < 4; ++s) { sc[0] = mfma(qf[s], kf[0][s], sc[0]); sc[1] = mfma(qf[s], kf[1][s], sc[1]); }
      FENCE();
    }
  }
#pragma unroll
  for (int j = 0; j < 8; ++j) LOAD_V(3, j);
  FENCE();
  u32x4 wo[8];
#pragma unroll
  for (int j = 0; j < 8; ++j) wo[j] = __builtin_amdgcn_raw_buffer_load_b128(rs_wo, g * (8 * 1024) + c * 16, j * 1024, STREAM_AUX);
  FENCE();
  STAMP(5);
  float sv[8];
#pragma unroll
  for (int i = 0; i < 8; ++i) sv[i] = rbf(rbf(sc[i >> 2][i & 3]) * SCALE);
  float m = fmaxf(fmaxf(fmaxf(sv[0], sv[1]), fmaxf(sv[2], sv[3])), fmaxf(fmaxf(sv[4], sv[5]), fmaxf(sv[6], sv[7])));
  m = row_max(m);
  if (c == 0) *(LAS float*)(L_MAX + g * 16 + wave * 4) = m;
  WG_BARRIER();
  {
    const f32x4 mm = __builtin_bit_cast(f32x4, lds_r128(L_MAX + g * 16));
    m = fmaxf(fmaxf(mm.x, mm.y), fmaxf(mm.z, mm.w));
  }
  float l = 0.f;
#pragma unroll
  for (int i = 0; i < 8; ++i) { sv[i] = __builtin_amdgcn_exp2f((sv[i] - m) * LOG2E); l += sv[i]; }
  l = row_sum(l);
  if (c == 0) *(LAS float*)(L_SUM + g * 16 + wave * 4) = l;
  const u32 pw = L_P + wave * 1280;
  lds_w128(pw + g * 320 + c * 16, u32x4{pk(sv[0], sv[1]), pk(sv[2], sv[3]), pk(sv[4], sv[5]), pk(sv[6], sv[7])});
  FENCE();
  STAMP(6);
  f32x4 acc[2] = {{0.f, 0.f, 0.f, 0.f}, {0.f, 0.f, 0.f, 0.f}};
#pragma unroll
  for (int u = 0; u < 4; ++u) {
    u32x4 am[4];
#pragma unroll
    for (int t = 0; t < 4; ++t) am[t] = lds_r128((((c & 3) == t) ? pw + (c >> 2) * 320 + g * 16 : L_ZERO) + u * 64);
    tile_mma(am, v[u], acc);
    FENCE();
  }
  STAMP(7);
  lds_w128(L_MRG1 + wave * 2048 + lane * 16, __builtin_bit_cast(u32x4, acc[0]));
  lds_w128(L_MRG1 + wave * 2048 + 1024 + lane * 16, __builtin_bit_cast(u32x4, acc[1]));
  WG_BARRIER();
  if (wave < 2) {
    const u32 a = L_MRG1 + wave * 1024 + lane * 16;
    const f32x4 o = (__builtin_bit_cast(f32x4, lds_r128(a)) + __builtin_bit_cast(f32x4, lds_r128(a + 2048))) +
                    (__builtin_bit_cast(f32x4, lds_r128(a + 4096)) + __builtin_bit_cast(f32x4, lds_r128(a + 6144)));
    __builtin_amdgcn_raw_buffer_store_b128(clean(o), rs_ws, lane * 16, WS_OP + cu * 2048 + wave * 1024, AUX_SC1);
  } else if (wave == 2) {
    const f32x4 ls = __builtin_bit_cast(f32x4, lds_r128(L_SUM + g * 16));
    const float lt = (ls.x + ls.y) + (ls.z + ls.w);
    if (c == 0) __builtin_amdgcn_raw_buffer_store_b64(u32x2{min(asu(m), SENT - 1), min(asu(lt), SENT - 1)}, rs_ws, g * 8, WS_ML + cu * 32, AUX_SC1);
  }
  FENCE();
  STAMP(8);
  u32x4 bf[8] = {bfrag<0>(wo), bfrag<1>(wo), bfrag<2>(wo), bfrag<3>(wo), bfrag<4>(wo), bfrag<5>(wo), bfrag<6>(wo), bfrag<7>(wo)};
#pragma unroll
  for (int j = 0; j < 8; ++j) asm volatile("" : "+v"(bf[j]));
  FENCE();
  STAMP(9);
  u32x4 o0, o1; u32x2 ml;
  for (u32 spin = 0; spin < SPIN_CAP; ++spin) {
    asm volatile("" ::: "memory");
    o0 = __builtin_amdgcn_raw_buffer_load_b128(rs_ws, c * 2048 + g * 16, WS_OP + grp * 32768 + (hi2 * 16 + wave * 4) * 16, AUX_SC1);
    o1 = __builtin_amdgcn_raw_buffer_load_b128(rs_ws, c * 2048 + g * 16 + 1024, WS_OP + grp * 32768 + (hi2 * 16 + wave * 4) * 16, AUX_SC1);
    ml = __builtin_amdgcn_raw_buffer_load_b64(rs_ws, c * 32, WS_ML + grp * 512 + hi2 * 8, AUX_SC1);
    if (__all(max(max(max4(o0), max4(o1)), max(ml.x, ml.y)) != SENT)) break;
  }
  FENCE();
  STAMP(10);
  f32x4 oa[2] = {{0.f, 0.f, 0.f, 0.f}, {0.f, 0.f, 0.f, 0.f}};
  {
    const float ms = asf(ml.x), ls = asf(ml.y);
    float M = fmaxf(ms, dpp<XOR1>(ms)); M = fmaxf(M, dpp<XOR2>(M));
    const float e = __builtin_amdgcn_exp2f((ms - M) * LOG2E);
    float L = e * ls; L += dpp<XOR1>(L); L += dpp<XOR2>(L);
    const float f = e / L;
    float y[8];
#pragma unroll
    for (int j = 0; j < 8; ++j) { float t = f * asf(j < 4 ? o0[j & 3] : o1[j & 3]); t += dpp<XOR1>(t); y[j] = t + dpp<XOR2>(t); }
    u32x4 am[4];
    mask4(u32x4{pk(y[0], y[1]), pk(y[2], y[3]), pk(y[4], y[5]), pk(y[6], y[7])}, lane, am);
#pragma unroll
    for (int t = 0; t < 4; ++t) { oa[0] = mfma(am[t], bf[t], oa[0]); oa[1] = mfma(am[t], bf[4 + t], oa[1]); }
  }
  lds_w128(L_MRG2 + wave * 2048 + lane * 16, __builtin_bit_cast(u32x4, oa[0]));
  lds_w128(L_MRG2 + wave * 2048 + 1024 + lane * 16, __builtin_bit_cast(u32x4, oa[1]));
  WG_BARRIER();
  STAMP(11);
  if (wave < 2) {
    const u32 ln = wave * 32 + (lane >> 1), a = L_MRG2 + (lane & 1) * 1024 + ln * 16;
    const f32x4 o = (__builtin_bit_cast(f32x4, lds_r128(a)) + __builtin_bit_cast(f32x4, lds_r128(a + 2048))) +
                    (__builtin_bit_cast(f32x4, lds_r128(a + 4096)) + __builtin_bit_cast(f32x4, lds_r128(a + 6144)));
    __builtin_amdgcn_raw_buffer_store_b128(clean(o), rs_ws, ln * 2048 + (lane & 1) * 16, WS_FP + lo2 * (64 * 2048) + (grp * 4 + hi2) * 32, AUX_SC1);
  } else if (wave == 3) {
    u32x4 pa, pb;
    for (u32 spin = 0; spin < SPIN_CAP; ++spin) {
      asm volatile("" ::: "memory");
      pa = __builtin_amdgcn_raw_buffer_load_b128(rs_ws, lane * 16, WS_FP + cu * 2048, AUX_SC1);
      pb = __builtin_amdgcn_raw_buffer_load_b128(rs_ws, lane * 16 + 1024, WS_FP + cu * 2048, AUX_SC1);
      if (__all(max(max4(pa), max4(pb)) != SENT)) break;
    }
    FENCE();
    STAMP(12);
    const float gt = *(volatile LAS float*)(L_CTL + 4), gn = rbf(1.f - gt);
    float y[4];
#pragma unroll
    for (int i = 0; i < 4; ++i) {
      float a = asf(pa[i]), b = asf(pb[i]);
      a += dpp<XOR2>(a); a += dpp<ROR4>(a); a += dpp<ROR8>(a); a = xrow_sum(a);
      b += dpp<XOR2>(b); b += dpp<ROR4>(b); b += dpp<ROR8>(b); b = xrow_sum(b);
      y[i] = rbf(gt * rbf(a)) + rbf(gn * rbf(b));
    }
    const rsrc_t rs_out = mk(out, 4096);
    if (lane < 2) __builtin_amdgcn_raw_buffer_store_b64(u32x2{pk(y[0], y[1]), pk(y[2], y[3])}, rs_out, lane * 8, ((cu >> 4) & 3) * 1024 + (cu >> 6) * 256 + (cu & 15) * 16, 0);
  }
  STAMP(13);
  if (wave == 3 && id == PBLK) {
    u32 d;
    if (PROBE == 100) d = (u32)(__builtin_amdgcn_s_memtime() - c0) / 24u;
    else if (PROBE >= 50) d = __builtin_amdgcn_readfirstlane(*(volatile LAS u32*)(L_CTL + 16 + 4 * (PROBE - 50)));
    else d = (u32)(ts[PROBE] - ts[0]);
    d = d > 5000u ? 5000u : d;
    const u64 until = ts[13] + (u64)d * AMP;
    while (__builtin_amdgcn_s_memrealtime() < until) __builtin_amdgcn_s_sleep(1);
  }
  STAMP_OUT(14);
}

extern "C" void kernel_launch(void* const* d_in, const int* in_sizes, int n_in, void* d_out, int out_size, void* d_ws, size_t ws_size,
                              hipStream_t stream) {
  (void)in_sizes; (void)n_in; (void)out_size; (void)ws_size;
  (void)hipFuncSetAttribute((const void*)gated_mix_decode_kernel, hipFuncAttributeMaxDynamicSharedMemorySize, (int)L_END);
  hipLaunchKernelGGL(gated_mix_decode_kernel, dim3(256), dim3(320), L_END, stream, (const char*)d_in[0], (const char*)d_in[1], (const char*)d_in[2],
                     (const char*)d_in[3], (const char*)d_in[4], (const char*)d_in[6], (const char*)d_in[7], (const char*)d_in[8],
                     (const char*)d_in[9], (const char*)d_in[10], (char*)d_out, (char*)d_ws);
}
```

```cpp
#define PROBE 0
#define AMP 4
#define PBLK 0
#include <hip/hip_runtime.h>
#include <hip/hip_bf16.h>
#include <stdint.h>

typedef unsigned u32;
typedef unsigned long long u64;
typedef u32 u32x4 __attribute__((ext_vector_type(4)));
typedef u32 u32x2 __attribute__((ext_vector_type(2)));
typedef float f32x4 __attribute__((ext_vector_type(4)));
typedef float f32x2 __attribute__((ext_vector_type(2)));
typedef __bf16 bf16x8 __attribute__((ext_vector_type(8)));
typedef __bf16 bf16x2 __attribute__((ext_vector_type(2)));
typedef __amdgpu_buffer_rsrc_t rsrc_t;
#define LAS __attribute__((address_space(3)))

namespace {
constexpr int SK = 8192, WIN = 2048;
constexpr u32 WS_EPOCH = 0, WS_FLAG = 4096, WS_ML = 8192, WS_QP = 65536, WS_OP = WS_QP + 256 * 2048,
              WS_FP = WS_OP + 256 * 2048, WS_T0 = WS_FP + 256 * 2048, WS_T1 = WS_T0 + 4096, WS_END = WS_T1 + 4096;
constexpr u32 L_K = 0, L_Q = 131072, L_ZERO = L_Q + 1280, L_P = L_ZERO + 256, L_MAX = L_P + 4 * 1280, L_SUM = L_MAX + 64,
              L_CTL = L_SUM + 64, L_MRG1 = L_CTL + 64, L_MRG2 = L_MRG1 + 8192, L_END = L_MRG2 + 8192;
constexpr u32 SENT = 0xFFFFFFFFu;
constexpr int AUX_NT = 2, AUX_SC1 = 16;
#define STREAM_AUX AUX_NT
constexpr u32 SPIN_CAP = 1u << 15;
constexpr float SCALE = 0.08838834764831845f, LOG2E = 1.4426950408889634f;

__device__ __forceinline__ float asf(u32 v) { return __builtin_bit_cast(float, v); }
__device__ __forceinline__ u32 asu(float v) { return __builtin_bit_cast(u32, v); }
__device__ __forceinline__ rsrc_t mk(const void* p, u32 bytes) { return __builtin_amdgcn_make_buffer_rsrc((void*)p, (short)0, (int)bytes, 0x00020000); }
__device__ __forceinline__ u32 pk(float a, float b) { const bf16x2 v = __builtin_convertvector(f32x2{a, b}, bf16x2); return __builtin_bit_cast(u32, v); }
__device__ __forceinline__ float rbf(float a) { return asf(pk(a, a) & 0xffff0000u); }
__device__ __forceinline__ float lo2f(u32 v) { return asf(v << 16); }
__device__ __forceinline__ float hi2f(u32 v) { return asf(v & 0xffff0000u); }
template <int CTRL> __device__ __forceinline__ float dpp(float x) {
  return asf((u32)__builtin_amdgcn_mov_dpp((int)asu(x), CTRL, 0xf, 0xf, true));
}
constexpr int XOR1 = 0xB1, XOR2 = 0x4E, ROR4 = 0x124, ROR8 = 0x128;
__device__ __forceinline__ float row_max(float x) { x = fmaxf(x, dpp<XOR1>(x)); x = fmaxf(x, dpp<XOR2>(x)); x = fmaxf(x, dpp<ROR4>(x)); return fmaxf(x, dpp<ROR8>(x)); }
__device__ __forceinline__ float row_sum(float x) { x += dpp<XOR1>(x); x += dpp<XOR2>(x); x += dpp<ROR4>(x); return x + dpp<ROR8>(x); }
__device__ __forceinline__ float xrow_sum(float x) {
  auto s = __builtin_amdgcn_permlane16_swap(asu(x), asu(x), false, false);
  x = asf(s[0]) + asf(s[1]);
  auto t = __builtin_amdgcn_permlane32_swap(asu(x), asu(x), false, false);
  return asf(t[0]) + asf(t[1]);
}
__device__ __forceinline__ f32x4 mfma(u32x4 a, u32x4 b, f32x4 c) {
  return __builtin_amdgcn_mfma_f32_16x16x32_bf16(__builtin_bit_cast(bf16x8, a), __builtin_bit_cast(bf16x8, b), c, 0, 0, 0);
}
template <int J> __device__ __forceinline__ u32x4 bfrag(const u32x4 (&t)[8]) {
  constexpr u32 sel = (J & 1) ? 0x07060302u : 0x05040100u;
  return u32x4{__builtin_amdgcn_perm(t[1][J >> 1], t[0][J >> 1], sel), __builtin_amdgcn_perm(t[3][J >> 1], t[2][J >> 1], sel),
               __builtin_amdgcn_perm(t[5][J >> 1], t[4][J >> 1], sel), __builtin_amdgcn_perm(t[7][J >> 1], t[6][J >> 1], sel)};
}
__device__ __forceinline__ void tile_mma(const u32x4 (&am)[4], const u32x4 (&t)[8], f32x4 (&acc)[2]) {
  acc[0] = mfma(am[0], bfrag<0>(t), acc[0]); acc[1] = mfma(am[0], bfrag<4>(t), acc[1]);
  acc[0] = mfma(am[1], bfrag<1>(t), acc[0]); acc[1] = mfma(am[1], bfrag<5>(t), acc[1]);
  acc[0] = mfma(am[2], bfrag<2>(t), acc[0]); acc[1] = mfma(am[2], bfrag<6>(t), acc[1]);
  acc[0] = mfma(am[3], bfrag<3>(t), acc[0]); acc[1] = mfma(am[3], bfrag<7>(t), acc[1]);
}
__device__ __forceinline__ void mask4(u32x4 a, int lane, u32x4 (&am)[4]) {
#pragma unroll
  for (int t = 0; t < 4; ++t) am[t] = ((lane & 3) == t) ? a : u32x4{0u, 0u, 0u, 0u};
}
__device__ __forceinline__ u32x4 clean(f32x4 v) {
  const u32x4 u = __builtin_bit_cast(u32x4, v);
  return u32x4{min(u.x, SENT - 1), min(u.y, SENT - 1), min(u.z, SENT - 1), min(u.w, SENT - 1)};
}
__device__ __forceinline__ u32 max4(u32x4 v) { return max(max(v.x, v.y), max(v.z, v.w)); }
__device__ __forceinline__ u32x4 lds_r128(u32 addr) { return *(const LAS u32x4*)addr; }
__device__ __forceinline__ void lds_w128(u32 addr, u32x4 v) { *(LAS u32x4*)addr = v; }
#define FENCE() do { asm volatile("" ::: "memory"); __builtin_amdgcn_sched_barrier(0); } while (0)
#define WG_BARRIER() do { __builtin_amdgcn_sched_barrier(0); asm volatile("s_waitcnt lgkmcnt(0)\n\ts_barrier" ::: "memory"); __builtin_amdgcn_sched_barrier(0); } while (0)
#define STAMP(i) do { __builtin_amdgcn_sched_barrier(0); asm volatile("s_memrealtime %0\n\ts_waitcnt lgkmcnt(0)" : "=s"(ts[i]) :: "memory"); __builtin_amdgcn_sched_barrier(0); } while (0)
#define STAMP_OUT(n)
}

__global__ void __launch_bounds__(320, 2)
gated_mix_decode_kernel(const char* x, const char* kA, const char* vA, const char* wqA, const char* woA, const char* kB, const char* vB,
                        const char* wqB, const char* woB, const char* gw, char* out, char* ws) {
  extern __shared__ __attribute__((aligned(16))) char lds_[];
  const int lane = threadIdx.x & 63, wave = __builtin_amdgcn_readfirstlane(threadIdx.x >> 6);
  const int g = lane >> 4, c = lane & 15;
  const u32 id = blockIdx.x, r5 = id >> 3;
  const u32 grp = (id & 7) * 2 + (r5 >> 4), gi = r5 & 15, cu = grp * 16 + gi;
  const u32 br = grp >> 3, kvh = grp & 7, hi2 = gi >> 2, lo2 = gi & 3;
  const rsrc_t rs_ws = mk(ws, WS_END);
  u64 ts[16] = {};
  const u64 c0 = __builtin_amdgcn_s_memtime();
  STAMP(0);

  if (wave == 4) {
    __builtin_amdgcn_s_setprio(3);
    const rsrc_t rs_x = mk(x, 4096), rs_gw = mk(gw, 1024);
    const rsrc_t rs_wq = mk((br ? wqB : wqA) + (size_t)lo2 * (128 * 8192) + kvh * 1024 + hi2 * 256, 127 * 8192 + 256);
    const u32x2 ep = __builtin_amdgcn_raw_buffer_load_b64(rs_ws, 0, WS_EPOCH, AUX_SC1);
    u32x4 xa[4], w[4][8];
#pragma unroll
    for (int s = 0; s < 4; ++s) xa[s] = __builtin_amdgcn_raw_buffer_load_b128(rs_x, (c >> 2) * 1024 + g * 16 + s * 64, lo2 * 256, 0);
    FENCE();
#pragma unroll
    for (int s = 0; s < 4; ++s)
#pragma unroll
      for (int j = 0; j < 8; ++j) w[s][j] = __builtin_amdgcn_raw_buffer_load_b128(rs_wq, g * (8 * 8192) + c * 16, (32 * s + j) * 8192, STREAM_AUX);
    FENCE();
    if (lane < 16) lds_w128(L_ZERO + lane * 16, u32x4{0u, 0u, 0u, 0u});
    if (lane == 0) *(volatile LAS u32*)(L_CTL) = 0u;
    WG_BARRIER();
    STAMP(1);
    const u32x4 gx = __builtin_amdgcn_raw_buffer_load_b128(rs_x, lane * 16, ((cu >> 4) & 3) * 1024, 0);
    const u32x4 gv = __builtin_amdgcn_raw_buffer_load_b128(rs_gw, lane * 16, 0, 0);
    {
      const u32x4 s4 = {SENT, SENT, SENT, SENT};
      __builtin_amdgcn_raw_buffer_store_b128(s4, rs_ws, lane * 16, WS_OP + cu * 2048, AUX_SC1);
      __builtin_amdgcn_raw_buffer_store_b128(s4, rs_ws, lane * 16 + 1024, WS_OP + cu * 2048, AUX_SC1);
      __builtin_amdgcn_raw_buffer_store_b128(s4, rs_ws, lane * 16, WS_FP + cu * 2048, AUX_SC1);
      __builtin_amdgcn_raw_buffer_store_b128(s4, rs_ws, lane * 16 + 1024, WS_FP + cu * 2048, AUX_SC1);
      if (lane < 2) __builtin_amdgcn_raw_buffer_store_b128(s4, rs_ws, lane * 16, WS_ML + cu * 32, AUX_SC1);
    }
    FENCE();
    f32x4 acc[2] = {{0.f, 0.f, 0.f, 0.f}, {0.f, 0.f, 0.f, 0.f}};
#pragma unroll
    for (int s = 0; s < 4; ++s) { u32x4 am[4]; mask4(xa[s], lane, am); tile_mma(am, w[s], acc); }
    __builtin_amdgcn_raw_buffer_store_b128(__builtin_bit_cast(u32x4, acc[0]), rs_ws, lane * 16, WS_QP + cu * 2048, AUX_SC1);
    __builtin_amdgcn_raw_buffer_store_b128(__builtin_bit_cast(u32x4, acc[1]), rs_ws, lane * 16 + 1024, WS_QP + cu * 2048, AUX_SC1);
    STAMP(2);
    if (lane == 0) __builtin_amdgcn_raw_buffer_store_b32((u32)ts[0], rs_ws, 0, WS_T0 + id * 4, AUX_SC1);
    const u64 tag64 = (((u64)ep.y << 32) | ep.x) + 1;
    const u32x2 tag = {(u32)tag64, (u32)(tag64 >> 32)};
    asm volatile("s_waitcnt vmcnt(0)" ::: "memory");
    if (lane == 0) __builtin_amdgcn_raw_buffer_store_b64(tag, rs_ws, 0, WS_FLAG + cu * 8, AUX_SC1);
    STAMP(3);
    {
      float z = 0.f;
#pragma unroll
      for (int i = 0; i < 4; ++i) z += rbf(lo2f(gx[i]) * lo2f(gv[i])) + rbf(hi2f(gx[i]) * hi2f(gv[i]));
      z = rbf(xrow_sum(row_sum(z)));
      const float gt = rbf(1.f / (1.f + __expf(-z)));
      if (lane == 0) *(volatile LAS float*)(L_CTL + 4) = gt;
    }
    for (u32 spin = 0; spin < SPIN_CAP; ++spin) {
      asm volatile("" ::: "memory");
      const u32x4 f0 = __builtin_amdgcn_raw_buffer_load_b128(rs_ws, lane * 16, WS_FLAG, AUX_SC1);
      const u32x4 f1 = __builtin_amdgcn_raw_buffer_load_b128(rs_ws, lane * 16 + 1024, WS_FLAG, AUX_SC1);
      const bool ok = f0.x == tag.x && f0.y == tag.y && f0.z == tag.x && f0.w == tag.y && f1.x == tag.x && f1.y == tag.y && f1.z == tag.x && f1.w == tag.y;
      if (__all(ok)) break;
      __builtin_amdgcn_s_sleep(4);
    }
    FENCE();
    STAMP(4);
    {
      u32x4 p[4][2];
#pragma unroll
      for (int kq = 0; kq < 4; ++kq)
#pragma unroll
        for (int a = 0; a < 2; ++a)
          p[kq][a] = __builtin_amdgcn_raw_buffer_load_b128(rs_ws, g * 8192 + c * 16 + a * 1024, WS_QP + (grp * 16 + kq) * 2048 + hi2 * 256, AUX_SC1);
      f32x4 q[2];
#pragma unroll
      for (int a = 0; a < 2; ++a)
        q[a] = (__builtin_bit_cast(f32x4, p[0][a]) + __builtin_bit_cast(f32x4, p[1][a])) + (__builtin_bit_cast(f32x4, p[2][a]) + __builtin_bit_cast(f32x4, p[3][a]));
      lds_w128(L_Q + g * 320 + c * 16, u32x4{pk(q[0].x, q[0].y), pk(q[0].z, q[0].w), pk(q[1].x, q[1].y), pk(q[1].z, q[1].w)});
    }
    if (lane == 0) for (int i = 0; i < 5; ++i) *(volatile LAS u32*)(L_CTL + 16 + 4 * i) = (u32)(ts[i] - ts[0]);
    asm volatile("s_waitcnt lgkmcnt(0)" ::: "memory");
    if (lane == 0) *(volatile LAS u32*)(L_CTL) = 1u;
    if (id == 0 && lane == 0) __builtin_amdgcn_raw_buffer_store_b64(tag, rs_ws, 0, WS_EPOCH, AUX_SC1);
    STAMP(5);
    STAMP_OUT(6);
    return;
  }

  const size_t kvoff = ((size_t)(hi2 * 8 + kvh) * SK + (SK - WIN) + lo2 * 512) * 256 + wave * 1024;
  const rsrc_t rs_k = mk((br ? kB : kA) + kvoff, 131072 - wave * 1024), rs_v = mk((br ? vB : vA) + kvoff, 131072 - wave * 1024);
  const rsrc_t rs_wo = mk((br ? woB : woA) + (size_t)((kvh * 4 + hi2) * 128 + wave * 32) * 1024 + lo2 * 256, 31 * 1024 + 256);
  const u32 rot = r5 & 31;
  const u32 kw = L_K + wave * 32768;
  u32x4 pe = __builtin_amdgcn_raw_buffer_load_b128(rs_ws, lane * 16, WS_T1, AUX_SC1);
  asm volatile("s_waitcnt vmcnt(0)" : "+v"(pe) :: "memory");
  WG_BARRIER();
  STAMP(1);
  {
    const u32 voff0 = g * 256 + ((c ^ g) << 4);
#pragma unroll
    for (int p = 0; p < 32; ++p) {
      const u32 so = ((p + rot) & 31) << 12, m0v = kw + p * 1024, vo = voff0 ^ ((p >> 3) << 6);
      if (STREAM_AUX) asm volatile("s_mov_b32 m0, %0\n\ts_nop 0\n\tbuffer_load_dwordx4 %1, %2, %3 offen nt lds" :: "s"(m0v), "v"(vo), "s"(rs_k), "s"(so) : "memory");
      else asm volatile("s_mov_b32 m0, %0\n\ts_nop 0\n\tbuffer_load_dwordx4 %1, %2, %3 offen lds" :: "s"(m0v), "v"(vo), "s"(rs_k), "s"(so) : "memory");
    }
  }
  FENCE();
  STAMP(2);
  u32x4 v[4][8];
#define LOAD_V(u, j) v[u][j] = __builtin_amdgcn_raw_buffer_load_b128(rs_v, lane * 16, ((8 * (u) + (j) + rot) & 31) << 12, STREAM_AUX)
#pragma unroll
  for (int j = 0; j < 8; ++j) LOAD_V(0, j);
#pragma unroll
  for (int j = 0; j < 8; ++j) LOAD_V(1, j);
  FENCE();
  STAMP(3);
  for (u32 spin = 0; spin < (SPIN_CAP << 6); ++spin) {
    if (*(volatile LAS u32*)(L_CTL)) break;
    __builtin_amdgcn_s_sleep(2);
  }
  asm volatile("s_waitcnt vmcnt(16)" ::: "memory");
  FENCE();
  STAMP(4);
  f32x4 sc[2] = {{0.f, 0.f, 0.f, 0.f}, {0.f, 0.f, 0.f, 0.f}};
  {
    const u32 ka = kw + (c >> 2) * 8192 + (c & 3) * 256 + ((g ^ c) << 4);
#pragma unroll
    for (int t = 0; t < 4; ++t) {
      const u32 qa = ((c & 3) == t) ? L_Q + (c >> 2) * 320 + g * 16 : L_ZERO;
      u32x4 qf[4], kf[2][4];
#pragma unroll
      for (int s = 0; s < 4; ++s) qf[s] = lds_r128(qa + s * 64);
#pragma unroll
      for (int a = 0; a < 2; ++a)
#pragma unroll
        for (int s = 0; s < 4; ++s) kf[a][s] = lds_r128((ka ^ (s << 6)) + (4 * a + t) * 1024);
      LOAD_V(2, 2 * t); LOAD_V(2, 2 * t + 1);
#pragma unroll
      for (int s = 0; s < 4; ++s) { sc[0] = mfma(qf[s], kf[0][s], sc[0]); sc[1] = mfma(qf[s], kf[1][s], sc[1]); }
      FENCE();
    }
  }
#pragma unroll
  for (int j = 0; j < 8; ++j) LOAD_V(3, j);
  FENCE();
  u32x4 wo[8];
#pragma unroll
  for (int j = 0; j < 8; ++j) wo[j] = __builtin_amdgcn_raw_buffer_load_b128(rs_wo, g * (8 * 1024) + c * 16, j * 1024, STREAM_AUX);
  FENCE();
  STAMP(5);
  float sv[8];
#pragma unroll
  for (int i = 0; i < 8; ++i) sv[i] = rbf(rbf(sc[i >> 2][i & 3]) * SCALE);
  float m = fmaxf(fmaxf(fmaxf(sv[0], sv[1]), fmaxf(sv[2], sv[3])), fmaxf(fmaxf(sv[4], sv[5]), fmaxf(sv[6], sv[7])));
  m = row_max(m);
  if (c == 0) *(LAS float*)(L_MAX + g * 16 + wave * 4) = m;
  WG_BARRIER();
  {
    const f32x4 mm = __builtin_bit_cast(f32x4, lds_r128(L_MAX + g * 16));
    m = fmaxf(fmaxf(mm.x, mm.y), fmaxf(mm.z, mm.w));
  }
  float l = 0.f;
#pragma unroll
  for (int i = 0; i < 8; ++i) { sv[i] = __builtin_amdgcn_exp2f((sv[i] - m) * LOG2E); l += sv[i]; }
  l = row_sum(l);
  if (c == 0) *(LAS float*)(L_SUM + g * 16 + wave * 4) = l;
  const u32 pw = L_P + wave * 1280;
  lds_w128(pw + g * 320 + c * 16, u32x4{pk(sv[0], sv[1]), pk(sv[2], sv[3]), pk(sv[4], sv[5]), pk(sv[6], sv[7])});
  FENCE();
  STAMP(6);
  f32x4 acc[2] = {{0.f, 0.f, 0.f, 0.f}, {0.f, 0.f, 0.f, 0.f}};
#pragma unroll
  for (int u = 0; u < 4; ++u) {
    u32x4 am[4];
#pragma unroll
    for (int t = 0; t < 4; ++t) am[t] = lds_r128((((c & 3) == t) ? pw + (c >> 2) * 320 + g * 16 : L_ZERO) + u * 64);
    tile_mma(am, v[u], acc);
    FENCE();
  }
  STAMP(7);
  lds_w128(L_MRG1 + wave * 2048 + lane * 16, __builtin_bit_cast(u32x4, acc[0]));
  lds_w128(L_MRG1 + wave * 2048 + 1024 + lane * 16, __builtin_bit_cast(u32x4, acc[1]));
  WG_BARRIER();
  if (wave < 2) {
    const u32 a = L_MRG1 + wave * 1024 + lane * 16;
    const f32x4 o = (__builtin_bit_cast(f32x4, lds_r128(a)) + __builtin_bit_cast(f32x4, lds_r128(a + 2048))) +
                    (__builtin_bit_cast(f32x4, lds_r128(a + 4096)) + __builtin_bit_cast(f32x4, lds_r128(a + 6144)));
    __builtin_amdgcn_raw_buffer_store_b128(clean(o), rs_ws, lane * 16, WS_OP + cu * 2048 + wave * 1024, AUX_SC1);
  } else if (wave == 2) {
    const f32x4 ls = __builtin_bit_cast(f32x4, lds_r128(L_SUM + g * 16));
    const float lt = (ls.x + ls.y) + (ls.z + ls.w);
    if (c == 0) __builtin_amdgcn_raw_buffer_store_b64(u32x2{min(asu(m), SENT - 1), min(asu(lt), SENT - 1)}, rs_ws, g * 8, WS_ML + cu * 32, AUX_SC1);
  }
  FENCE();
  STAMP(8);
  u32x4 bf[8] = {bfrag<0>(wo), bfrag<1>(wo), bfrag<2>(wo), bfrag<3>(wo), bfrag<4>(wo), bfrag<5>(wo), bfrag<6>(wo), bfrag<7>(wo)};
#pragma unroll
  for (int j = 0; j < 8; ++j) asm volatile("" : "+v"(bf[j]));
  FENCE();
  STAMP(9);
  u32x4 o0, o1; u32x2 ml;
  for (u32 spin = 0; spin < SPIN_CAP; ++spin) {
    asm volatile("" ::: "memory");
    o0 = __builtin_amdgcn_raw_buffer_load_b128(rs_ws, c * 2048 + g * 16, WS_OP + grp * 32768 + (hi2 * 16 + wave * 4) * 16, AUX_SC1);
    o1 = __builtin_amdgcn_raw_buffer_load_b128(rs_ws, c * 2048 + g * 16 + 1024, WS_OP + grp * 32768 + (hi2 * 16 + wave * 4) * 16, AUX_SC1);
    ml = __builtin_amdgcn_raw_buffer_load_b64(rs_ws, c * 32, WS_ML + grp * 512 + hi2 * 8, AUX_SC1);
    if (__all(max(max(max4(o0), max4(o1)), max(ml.x, ml.y)) != SENT)) break;
  }
  FENCE();
  STAMP(10);
  f32x4 oa[2] = {{0.f, 0.f, 0.f, 0.f}, {0.f, 0.f, 0.f, 0.f}};
  {
    const float ms = asf(ml.x), ls = asf(ml.y);
    float M = fmaxf(ms, dpp<XOR1>(ms)); M = fmaxf(M, dpp<XOR2>(M));
    const float e = __builtin_amdgcn_exp2f((ms - M) * LOG2E);
    float L = e * ls; L += dpp<XOR1>(L); L += dpp<XOR2>(L);
    const float f = e / L;
    float y[8];
#pragma unroll
    for (int j = 0; j < 8; ++j) { float t = f * asf(j < 4 ? o0[j & 3] : o1[j & 3]); t += dpp<XOR1>(t); y[j] = t + dpp<XOR2>(t); }
    u32x4 am[4];
    mask4(u32x4{pk(y[0], y[1]), pk(y[2], y[3]), pk(y[4], y[5]), pk(y[6], y[7])}, lane, am);
#pragma unroll
    for (int t = 0; t < 4; ++t) { oa[0] = mfma(am[t], bf[t], oa[0]); oa[1] = mfma(am[t], bf[4 + t], oa[1]); }
  }
  lds_w128(L_MRG2 + wave * 2048 + lane * 16, __builtin_bit_cast(u32x4, oa[0]));
  lds_w128(L_MRG2 + wave * 2048 + 1024 + lane * 16, __builtin_bit_cast(u32x4, oa[1]));
  WG_BARRIER();
  STAMP(11);
  if (wave < 2) {
    const u32 ln = wave * 32 + (lane >> 1), a = L_MRG2 + (lane & 1) * 1024 + ln * 16;
    const f32x4 o = (__builtin_bit_cast(f32x4, lds_r128(a)) + __builtin_bit_cast(f32x4, lds_r128(a + 2048))) +
                    (__builtin_bit_cast(f32x4, lds_r128(a + 4096)) + __builtin_bit_cast(f32x4, lds_r128(a + 6144)));
    __builtin_amdgcn_raw_buffer_store_b128(clean(o), rs_ws, ln * 2048 + (lane & 1) * 16, WS_FP + lo2 * (64 * 2048) + (grp * 4 + hi2) * 32, AUX_SC1);
  } else if (wave == 3) {
    u32x4 pa, pb;
    for (u32 spin = 0; spin < SPIN_CAP; ++spin) {
      asm volatile("" ::: "memory");
      pa = __builtin_amdgcn_raw_buffer_load_b128(rs_ws, lane * 16, WS_FP + cu * 2048, AUX_SC1);
      pb = __builtin_amdgcn_raw_buffer_load_b128(rs_ws, lane * 16 + 1024, WS_FP + cu * 2048, AUX_SC1);
      if (__all(max(max4(pa), max4(pb)) != SENT)) break;
    }
    FENCE();
    STAMP(12);
    const float gt = *(volatile LAS float*)(L_CTL + 4), gn = rbf(1.f - gt);
    float y[4];
#pragma unroll
    for (int i = 0; i < 4; ++i) {
      float a = asf(pa[i]), b = asf(pb[i]);
      a += dpp<XOR2>(a); a += dpp<ROR4>(a); a += dpp<ROR8>(a); a = xrow_sum(a);
      b += dpp<XOR2>(b); b += dpp<ROR4>(b); b += dpp<ROR8>(b); b = xrow_sum(b);
      y[i] = rbf(gt * rbf(a)) + rbf(gn * rbf(b));
    }
    const rsrc_t rs_out = mk(out, 4096);
    if (lane < 2) __builtin_amdgcn_raw_buffer_store_b64(u32x2{pk(y[0], y[1]), pk(y[2], y[3])}, rs_out, lane * 8, ((cu >> 4) & 3) * 1024 + (cu >> 6) * 256 + (cu & 15) * 16, 0);
  }
  STAMP(13);
  if (wave == 3 && id == PBLK) {
    u32 d;
    const u32x4 st = __builtin_amdgcn_raw_buffer_load_b128(rs_ws, lane * 16, WS_T0, AUX_SC1);
    const u32 base = (u32)ts[0] - 100000u;
    u32 mn = min(min(st.x - base, st.y - base), min(st.z - base, st.w - base)), mx = max(max(st.x - base, st.y - base), max(st.z - base, st.w - base));
    u32 me = max(max(pe.x - base, pe.y - base), max(pe.z - base, pe.w - base));
    const u32 e0 = __builtin_amdgcn_readfirstlane(pe.x - base);
    u32 me_no0 = lane == 0 ? max(pe.y - base, max(pe.z - base, pe.w - base)) : me;
    for (int o = 1; o < 64; o <<= 1) { mn = min(mn, (u32)__shfl_xor((int)mn, o)); mx = max(mx, (u32)__shfl_xor((int)mx, o)); me = max(me, (u32)__shfl_xor((int)me, o)); me_no0 = max(me_no0, (u32)__shfl_xor((int)me_no0, o)); }
    mn = __builtin_amdgcn_readfirstlane(mn); mx = __builtin_amdgcn_readfirstlane(mx); me = __builtin_amdgcn_readfirstlane(me); me_no0 = __builtin_amdgcn_readfirstlane(me_no0);
    if (PROBE == 200) d = (u32)ts[13] - base - mn;
    else if (PROBE == 201) d = mx - mn;
    else if (PROBE == 202) { d = mn - me; if (d > 2000u) d = 0; }
    else if (PROBE == 203) d = 100000u - mn;
    else if (PROBE == 204) d = mx - 100000u + 200;
    else if (PROBE == 100) d = (u32)(__builtin_amdgcn_s_memtime() - c0) / 24u;
    else if (PROBE >= 50) d = __builtin_amdgcn_readfirstlane(*(volatile LAS u32*)(L_CTL + 16 + 4 * ((PROBE - 50) & 7)));
    else d = (u32)(ts[PROBE < 16 ? PROBE : 0] - ts[0]);
    d = d > 5000u ? 5000u : d;
    const u64 until = ts[13] + (u64)d * AMP;
    while (__builtin_amdgcn_s_memrealtime() < until) __builtin_amdgcn_s_sleep(1);
  }
  if (wave == 3 && lane == 0) __builtin_amdgcn_raw_buffer_store_b32((u32)__builtin_amdgcn_s_memrealtime(), rs_ws, 0, WS_T1 + id * 4, AUX_SC1);
  STAMP_OUT(14);
}

extern "C" void kernel_launch(void* const* d_in, const int* in_sizes, int n_in, void* d_out, int out_size, void* d_ws, size_t ws_size,
                              hipStream_t stream) {
  (void)in_sizes; (void)n_in; (void)out_size; (void)ws_size;
  (void)hipFuncSetAttribute((const void*)gated_mix_decode_kernel, hipFuncAttributeMaxDynamicSharedMemorySize, (int)L_END);
  hipLaunchKernelGGL(gated_mix_decode_kernel, dim3(256), dim3(320), L_END, stream, (const char*)d_in[0], (const char*)d_in[1], (const char*)d_in[2],
                     (const char*)d_in[3], (const char*)d_in[4], (const char*)d_in[6], (const char*)d_in[7], (const char*)d_in[8],
                     (const char*)d_in[9], (const char*)d_in[10], (char*)d_out, (char*)d_ws);
}
```

```cpp
#include <hip/hip_runtime.h>
#include <hip/hip_bf16.h>
#include <stdint.h>

typedef unsigned u32;
typedef unsigned long long u64;
typedef u32 u32x4 __attribute__((ext_vector_type(4)));
typedef u32 u32x2 __attribute__((ext_vector_type(2)));
typedef float f32x4 __attribute__((ext_vector_type(4)));
typedef float f32x2 __attribute__((ext_vector_type(2)));
typedef __bf16 bf16x8 __attribute__((ext_vector_type(8)));
typedef __bf16 bf16x2 __attribute__((ext_vector_type(2)));
typedef __amdgpu_buffer_rsrc_t rsrc_t;
#define LAS __attribute__((address_space(3)))

namespace {
constexpr int SK = 8192, WIN = 2048;
constexpr int A_DELAY = 6;
constexpr int LOOK_AT = 480;
constexpr int QPOS = 12;
constexpr int PV_LAG = 8;
constexpr int WO_PACED = 4;
constexpr int W_MIN = 4, W_MID = 6, W_MAX = 11, W_T0 = 65, W_D16 = 215, W_SHIFT = 4, W_EVERY = 4, W_BUSY = 2;
constexpr int FP_HOLD = 12;
constexpr u32 WS_EPOCH = 0, WS_FLAG = 4096, WS_ML = 8192, WS_QP = 65536, WS_OP = WS_QP + 256 * 1024, WS_FP = WS_OP + 256 * 1024,
              WS_END = WS_FP + 256 * 2048;
constexpr u32 L_K = 0, L_Q = 131072, L_ZERO = L_Q + 1280, L_P = L_ZERO + 256, L_MAX = L_P + 4 * 1280, L_SUM = L_MAX + 64,
              L_GATE = L_SUM + 64, L_JUNK = L_GATE + 64, L_END = L_JUNK + 256;
constexpr u32 L_MRG1 = 0, L_MRG2 = 8192;
constexpr u32 SENT = 0xFFFFFFFFu;
constexpr u32 OOB = 0x7FFFFFF0u;
constexpr int AUX_NT = 2, AUX_SC1 = 16;
constexpr u32 SPIN_CAP = 1u << 18;
constexpr float SCALE = 0.08838834764831845f, LOG2E = 1.4426950408889634f;

__device__ __forceinline__ float asf(u32 v) { return __builtin_bit_cast(float, v); }
__device__ __forceinline__ u32 asu(float v) { return __builtin_bit_cast(u32, v); }
__device__ __forceinline__ f32x4 asf4(u32x4 v) { return __builtin_bit_cast(f32x4, v); }
__device__ __forceinline__ u32x4 asu4(f32x4 v) { return __builtin_bit_cast(u32x4, v); }
__device__ __forceinline__ rsrc_t mk(const void* p, u32 bytes) { return __builtin_amdgcn_make_buffer_rsrc((void*)p, (short)0, (int)bytes, 0x00020000); }
__device__ __forceinline__ u32 pk(float a, float b) { const bf16x2 v = __builtin_convertvector(f32x2{a, b}, bf16x2); return __builtin_bit_cast(u32, v); }
__device__ __forceinline__ float rbf(float a) { return asf(pk(a, a) & 0xffff0000u); }
__device__ __forceinline__ float lo2f(u32 v) { return asf(v << 16); }
__device__ __forceinline__ float hi2f(u32 v) { return asf(v & 0xffff0000u); }
template <int CTRL> __device__ __forceinline__ float dpp(float x) { return asf((u32)__builtin_amdgcn_mov_dpp((int)asu(x), CTRL, 0xf, 0xf, true)); }
constexpr int XOR1 = 0xB1, XOR2 = 0x4E, XOR3 = 0x1B, XOR7 = 0x141, ROR4 = 0x124, ROR8 = 0x128;
__device__ __forceinline__ float row_max(float x) { x = fmaxf(x, dpp<XOR1>(x)); x = fmaxf(x, dpp<XOR2>(x)); x = fmaxf(x, dpp<ROR4>(x)); return fmaxf(x, dpp<ROR8>(x)); }
__device__ __forceinline__ float row_sum(float x) { x += dpp<XOR1>(x); x += dpp<XOR2>(x); x += dpp<ROR4>(x); return x + dpp<ROR8>(x); }
__device__ __forceinline__ float x16_sum(float x) {
  auto s = __builtin_amdgcn_permlane16_swap(asu(x), asu(x), false, false);
  return asf(s[0]) + asf(s[1]);
}
__device__ __forceinline__ float x32_sum(float x) {
  auto s = __builtin_amdgcn_permlane32_swap(asu(x), asu(x), false, false);
  return asf(s[0]) + asf(s[1]);
}
__device__ __forceinline__ f32x4 mfma(u32x4 a, u32x4 b, f32x4 c) {
  return __builtin_amdgcn_mfma_f32_16x16x32_bf16(__builtin_bit_cast(bf16x8, a), __builtin_bit_cast(bf16x8, b), c, 0, 0, 0);
}
template <int J> __device__ __forceinline__ u32x4 bfrag(const u32x4 (&t)[8]) {
  constexpr u32 sel = (J & 1) ? 0x07060302u : 0x05040100u;
  return u32x4{__builtin_amdgcn_perm(t[1][J >> 1], t[0][J >> 1], sel), __builtin_amdgcn_perm(t[3][J >> 1], t[2][J >> 1], sel),
               __builtin_amdgcn_perm(t[5][J >> 1], t[4][J >> 1], sel), __builtin_amdgcn_perm(t[7][J >> 1], t[6][J >> 1], sel)};
}
__device__ __forceinline__ void tile_mma(const u32x4 (&am)[4], const u32x4 (&t)[8], f32x4 (&acc)[2]) {
  acc[0] = mfma(am[0], bfrag<0>(t), acc[0]); acc[1] = mfma(am[0], bfrag<4>(t), acc[1]);
  acc[0] = mfma(am[1], bfrag<1>(t), acc[0]); acc[1] = mfma(am[1], bfrag<5>(t), acc[1]);
  acc[0] = mfma(am[2], bfrag<2>(t), acc[0]); acc[1] = mfma(am[2], bfrag<6>(t), acc[1]);
  acc[0] = mfma(am[3], bfrag<3>(t), acc[0]); acc[1] = mfma(am[3], bfrag<7>(t), acc[1]);
}
__device__ __forceinline__ u32x4 clean(u32x4 u) {
  return u32x4{min(u.x, SENT - 1), min(u.y, SENT - 1), min(u.z, SENT - 1), min(u.w, SENT - 1)};
}
__device__ __forceinline__ u32x4 lds_r128(u32 addr) { return *(const LAS u32x4*)(uintptr_t)addr; }
__device__ __forceinline__ void lds_w128(u32 addr, u32x4 v) { *(LAS u32x4*)(uintptr_t)addr = v; }
__device__ __forceinline__ void lds_w32(u32 addr, u32 v) { *(volatile LAS u32*)(uintptr_t)addr = v; }
__device__ __forceinline__ u32 lds_r32(u32 addr) { return *(volatile LAS u32*)(uintptr_t)addr; }
__device__ __forceinline__ u32x4 ld_dev(rsrc_t r, u32 vo, u32 so) { return __builtin_amdgcn_raw_buffer_load_b128(r, vo, so, AUX_SC1); }
__device__ __forceinline__ u32x2 ld_dev8(rsrc_t r, u32 vo, u32 so) { return __builtin_amdgcn_raw_buffer_load_b64(r, vo, so, AUX_SC1); }
__device__ __forceinline__ void st_dev(u32x4 v, rsrc_t r, u32 vo, u32 so) { __builtin_amdgcn_raw_buffer_store_b128(v, r, vo, so, AUX_SC1); }
__device__ __forceinline__ void st_dev8(u32x2 v, rsrc_t r, u32 vo, u32 so) { __builtin_amdgcn_raw_buffer_store_b64(v, r, vo, so, AUX_SC1); }
__device__ __forceinline__ u32x4 ld_stream(rsrc_t r, u32 vo, u32 so) { return __builtin_amdgcn_raw_buffer_load_b128(r, vo, so, AUX_NT); }
#define FENCE() do { asm volatile("" ::: "memory"); __builtin_amdgcn_sched_barrier(0); } while (0)
#define VMWAIT(n) do { __builtin_amdgcn_sched_barrier(0); asm volatile("s_waitcnt vmcnt(%0)" :: "i"(n) : "memory"); __builtin_amdgcn_sched_barrier(0); } while (0)
#define WG_BARRIER() do { __builtin_amdgcn_sched_barrier(0); asm volatile("s_waitcnt lgkmcnt(0)\n\ts_barrier" ::: "memory"); __builtin_amdgcn_sched_barrier(0); } while (0)
}

__global__ void __launch_bounds__(320, 2)
gated_mix_decode_kernel(const char* wqA, const char* wqB, const char* x, char* ws, const char* kA, const char* kB, const char* gw,
                        const char* vA, const char* vB, const char* woA, const char* woB, char* out) {
  const int lane = threadIdx.x & 63, wave = __builtin_amdgcn_readfirstlane(threadIdx.x >> 6);
  const int g = lane >> 4, c = lane & 15;
  const u32 id = blockIdx.x, xc = id & 7, r5 = id >> 3;
  const u32 ga = xc * 2 + (r5 >> 4), gi = r5 & 15, ab = gi >> 2, asp = gi & 3, cua = ga * 16 + gi;
  const u32 gp = (((xc & 3) + 1 + (xc >> 2) * 2) & 3) * 4 + (r5 >> 3), ph = (r5 >> 1) & 3, pq = (xc >> 2) * 2 + (r5 & 1);
  const u32 red = (pq ^ 2) * 64 + (xc & 3) * 16 + (r5 >> 1);
  const rsrc_t rs_ws = mk(ws, WS_END);

  if (__builtin_expect(wave == 4, 1)) {
    __builtin_amdgcn_s_setprio(3);
    const u64 t_start = __builtin_amdgcn_s_memrealtime();
    const rsrc_t rs_x = mk(x, 4096), rs_gw = mk(gw, 1024);
    const rsrc_t rs_wq = mk((gp >> 3 ? wqB : wqA) + (size_t)(pq >> 1) * (256 * 8192) + (gp & 7) * 1024 + ph * 256 + (pq & 1) * 128, 255 * 8192 + 128);
    const u32x2 ep = ld_dev8(rs_ws, 0, WS_EPOCH);
    u32x4 xa[4], w[4][8];
#pragma unroll
    for (int s = 0; s < 4; ++s) xa[s] = __builtin_amdgcn_raw_buffer_load_b128(rs_x, (c >> 2) * 1024 + g * 32 + (c & 2) * 8 + s * 128, (pq >> 1) * 512, 0);
    FENCE();
#pragma unroll
    for (int s = 0; s < 4; ++s)
#pragma unroll
      for (int j = 0; j < 8; ++j) w[s][j] = ld_stream(rs_wq, (lane >> 3) * (8 * 8192) + (lane & 7) * 16, (64 * s + j) * 8192);
    FENCE();
    const u32x4 gx = __builtin_amdgcn_raw_buffer_load_b128(rs_x, lane * 16, ((red >> 4) & 3) * 1024, 0);
    const u32x4 gv = __builtin_amdgcn_raw_buffer_load_b128(rs_gw, lane * 16, 0, 0);
    {
      const u32x4 s4 = {SENT, SENT, SENT, SENT};
      st_dev(s4, rs_ws, lane * 16, WS_OP + cua * 1024);
      st_dev(s4, rs_ws, lane * 16, WS_FP + red * 2048);
      st_dev(s4, rs_ws, lane * 16 + 1024, WS_FP + red * 2048);
      st_dev8(u32x2{SENT, SENT}, rs_ws, lane < 4 ? lane * 128 : OOB, WS_ML + ga * 512 + gi * 8);
    }
    FENCE();
    f32x4 acc[4] = {{0.f, 0.f, 0.f, 0.f}, {0.f, 0.f, 0.f, 0.f}, {0.f, 0.f, 0.f, 0.f}, {0.f, 0.f, 0.f, 0.f}};
#pragma unroll
    for (int s = 0; s < 4; ++s) {
      const u32x4 z4 = {0u, 0u, 0u, 0u}, a0 = (lane & 1) ? z4 : xa[s], a1 = (lane & 1) ? xa[s] : z4;
      acc[0] = mfma(a0, bfrag<0>(w[s]), acc[0]); acc[1] = mfma(a0, bfrag<2>(w[s]), acc[1]);
      acc[2] = mfma(a0, bfrag<4>(w[s]), acc[2]); acc[3] = mfma(a0, bfrag<6>(w[s]), acc[3]);
      acc[0] = mfma(a1, bfrag<1>(w[s]), acc[0]); acc[1] = mfma(a1, bfrag<3>(w[s]), acc[1]);
      acc[2] = mfma(a1, bfrag<5>(w[s]), acc[2]); acc[3] = mfma(a1, bfrag<7>(w[s]), acc[3]);
    }
    {
      const bool hi = lane & 8;
      float y[8];
#pragma unroll
      for (int j = 0; j < 8; ++j) { const float t = hi ? acc[j >> 1][2 + (j & 1)] : acc[j >> 1][j & 1]; y[j] = t + dpp<ROR8>(t); }
      const f32x4 o = hi ? f32x4{y[4], y[5], y[6], y[7]} : f32x4{y[0], y[1], y[2], y[3]};
      st_dev(asu4(o), rs_ws, lane * 16, WS_QP + (gp * 16 + ph * 4 + pq) * 1024);
    }
    const u64 tag64 = (((u64)ep.y << 32) | ep.x) + 1;
    const u32x2 tag = {(u32)tag64, (u32)(tag64 >> 32)};
    VMWAIT(0);
    st_dev8(tag, rs_ws, lane == 0 ? 0 : OOB, WS_FLAG + id * 8);
    FENCE();
    {
      float z = 0.f;
#pragma unroll
      for (int i = 0; i < 4; ++i) z += rbf(lo2f(gx[i]) * lo2f(gv[i])) + rbf(hi2f(gx[i]) * hi2f(gv[i]));
      z = rbf(x32_sum(x16_sum(row_sum(z))));
      const float gt = rbf(1.f / (1.f + __expf(-z)));
      if (lane == 0) lds_w32(L_GATE, asu(gt));
    }
    while (__builtin_amdgcn_s_memrealtime() - t_start < LOOK_AT) __builtin_amdgcn_s_sleep(2);
    for (u32 spin = 0; spin < SPIN_CAP; ++spin) {
      asm volatile("" ::: "memory");
      const u32x4 f0 = ld_dev(rs_ws, lane * 16, WS_FLAG), f1 = ld_dev(rs_ws, lane * 16 + 1024, WS_FLAG);
      const bool ok = f0.x == tag.x && f0.y == tag.y && f0.z == tag.x && f0.w == tag.y && f1.x == tag.x && f1.y == tag.y && f1.z == tag.x && f1.w == tag.y;
      if (__all(ok)) break;
      __builtin_amdgcn_s_sleep(16);
    }
    FENCE();
    {
      u32x4 p[2][2];
#pragma unroll
      for (int kh = 0; kh < 2; ++kh)
#pragma unroll
        for (int a = 0; a < 2; ++a) p[kh][a] = ld_dev(rs_ws, (g * 4 + (c >> 3)) * 1024 + (c & 7) * 16 + a * 128, WS_QP + ga * 16384 + kh * 2048 + ab * 256);
      const f32x4 q0 = asf4(p[0][0]) + asf4(p[1][0]), q1 = asf4(p[0][1]) + asf4(p[1][1]);
      lds_w128(L_Q + g * 320 + c * 16, u32x4{pk(q0.x, q0.y), pk(q0.z, q0.w), pk(q1.x, q1.y), pk(q1.z, q1.w)});
    }
    if (lane < 16) lds_w128(L_ZERO + lane * 16, u32x4{0u, 0u, 0u, 0u});
    WG_BARRIER();
    if (id == 0) st_dev8(tag, rs_ws, lane == 0 ? 0 : OOB, WS_EPOCH);
    return;
  }

  const size_t kvoff = ((size_t)(ab * 8 + (ga & 7)) * SK + (SK - WIN)) * 256 + wave * 1024;
  const rsrc_t rs_k = mk((ga >> 3 ? kB : kA) + kvoff, 524288 - wave * 1024), rs_v = mk((ga >> 3 ? vB : vA) + kvoff, 524288 - wave * 1024);
  const rsrc_t rs_wo = mk((gp >> 3 ? woB : woA) + (size_t)(((gp & 7) * 4 + ph) * 128 + wave * 32) * 1024 + pq * 256, 31 * 1024 + 256);
  const u32 rot = asp * 4 + (ga * 4 + ab) * 3;
#define KV_OFF(n) (((((n) >> 2) * 16 + ((n) & 3) + rot) & 127) << 12)
  const u32 kw = L_K + wave * 32768;
  if (wave == 1) {
    u64 pc; u32 len;
    asm volatile("s_getpc_b64 %0\n.Lgm_here:\n\ts_mov_b32 %1, .Lgm_last-.Lgm_here" : "=s"(pc), "=s"(len));
    const rsrc_t rs_c = mk((const void*)pc, len);
    asm volatile("s_mov_b32 m0, %0\n\ts_nop 0\n\tbuffer_load_dword %1, %2, 0 offen lds\n\tbuffer_load_dword %1, %2, %3 offen lds"
                 :: "s"(L_JUNK), "v"(lane * 128), "s"(rs_c), "s"(8192) : "memory");
  }
  __builtin_amdgcn_s_sleep(A_DELAY);
  const u32 t_start = (u32)__builtin_amdgcn_s_memrealtime();
  u64 wtab, wtgt;
  asm volatile("s_getpc_b64 %0\n\ts_branch .Lgm_wskip\n\t"
               "s_waitcnt vmcnt(0)\n\ts_setpc_b64 s[98:99]\n\ts_waitcnt vmcnt(1)\n\ts_setpc_b64 s[98:99]\n\t"
               "s_waitcnt vmcnt(2)\n\ts_setpc_b64 s[98:99]\n\ts_waitcnt vmcnt(3)\n\ts_setpc_b64 s[98:99]\n\t"
               "s_waitcnt vmcnt(4)\n\ts_setpc_b64 s[98:99]\n\ts_waitcnt vmcnt(5)\n\ts_setpc_b64 s[98:99]\n\t"
               "s_waitcnt vmcnt(6)\n\ts_setpc_b64 s[98:99]\n\ts_waitcnt vmcnt(7)\n\ts_setpc_b64 s[98:99]\n\t"
               "s_waitcnt vmcnt(8)\n\ts_setpc_b64 s[98:99]\n\ts_waitcnt vmcnt(9)\n\ts_setpc_b64 s[98:99]\n\t"
               "s_waitcnt vmcnt(10)\n\ts_setpc_b64 s[98:99]\n\ts_waitcnt vmcnt(11)\n\ts_setpc_b64 s[98:99]\n"
               ".Lgm_wskip:" : "=s"(wtab));
  static_assert(W_MIN >= 1 && W_MAX <= 12, "wait table");
  wtab -= 4;
  wtgt = wtab + 8 * W_MIN;
#define PACE(n, extra, update) do { __builtin_amdgcn_sched_barrier(0); \
    if (update) { \
      const int lag_ = (int)((u32)__builtin_amdgcn_s_memrealtime() - t_start) - (W_T0 + (n) * W_D16 / 16); \
      wtgt = wtab + 8 * (u32)min(max(W_MID + (extra) + (lag_ >> W_SHIFT), W_MIN), W_MAX); } \
    asm volatile("s_swappc_b64 s[98:99], %0" :: "s"(wtgt) : "s98", "s99", "memory"); __builtin_amdgcn_sched_barrier(0); } while (0)
  {
    const u32 voff0 = g * 256 + ((c ^ g) << 4);
#pragma unroll
    for (int p = 0; p < 32; ++p) {
      const u32 so = KV_OFF(p), m0v = kw + p * 1024, vo = voff0 ^ ((p >> 3) << 6);
      if (p >= W_MIN) PACE(p, 0, p % W_EVERY == 0);
      asm volatile("s_mov_b32 m0, %0\n\ts_nop 0\n\tbuffer_load_dwordx4 %1, %2, %3 offen nt lds" :: "s"(m0v), "v"(vo), "s"(rs_k), "s"(so) : "memory");
    }
  }
  FENCE();
  u32x4 v[4][8], wo[8];
#define SLOT_(n, extra, update) do { PACE(32 + (n), extra, update); \
    if ((n) < 32) v[((n) >> 3) & 3][(n) & 7] = ld_stream(rs_v, lane * 16, KV_OFF(n)); \
    else wo[(n) & 7] = ld_stream(rs_wo, g * (8 * 1024) + c * 16, ((n) & 7) * 1024); \
    FENCE(); } while (0)
#define SLOT_BUSY(n) SLOT_(n, W_BUSY, true)
#define SLOT_SAME(n) SLOT_(n, 0, false)
#define SLOTS(a, b) _Pragma("unroll") for (int n_ = (a); n_ < (b); ++n_) SLOT_(n_, 0, n_ % W_EVERY == 0)
  SLOTS(0, QPOS);
  WG_BARRIER();
  VMWAIT(QPOS);
  f32x4 sc[2] = {{0.f, 0.f, 0.f, 0.f}, {0.f, 0.f, 0.f, 0.f}};
  {
    const u32 ka = kw + (c >> 2) * 8192 + (c & 3) * 256 + ((g ^ c) << 4);
#pragma unroll
    for (int t = 0; t < 4; ++t) {
      const u32 qa = ((c & 3) == t) ? L_Q + (c >> 2) * 320 + g * 16 : L_ZERO;
      u32x4 qf[4], kf[2][4];
#pragma unroll
      for (int s = 0; s < 4; ++s) qf[s] = lds_r128(qa + s * 64);
#pragma unroll
      for (int a = 0; a < 2; ++a)
#pragma unroll
        for (int s = 0; s < 4; ++s) kf[a][s] = lds_r128((ka ^ (s << 6)) + (4 * a + t) * 1024);
      if (t == 0) SLOT_BUSY(QPOS); else SLOT_SAME(QPOS + 2 * t);
#pragma unroll
      for (int s = 0; s < 4; ++s) { sc[0] = mfma(qf[s], kf[0][s], sc[0]); sc[1] = mfma(qf[s], kf[1][s], sc[1]); }
      asm volatile("" : "+v"(sc[0]), "+v"(sc[1]));
      SLOT_SAME(QPOS + 2 * t + 1);
    }
  }
  float sv[8];
#pragma unroll
  for (int i = 0; i < 8; ++i) sv[i] = rbf(rbf(sc[i >> 2][i & 3]) * SCALE);
  float m = fmaxf(fmaxf(fmaxf(sv[0], sv[1]), fmaxf(sv[2], sv[3])), fmaxf(fmaxf(sv[4], sv[5]), fmaxf(sv[6], sv[7])));
  m = row_max(m);
  if (c == 0) lds_w32(L_MAX + g * 16 + wave * 4, asu(m));
  SLOT_BUSY(QPOS + 8);
  WG_BARRIER();
  {
    const f32x4 mm = asf4(lds_r128(L_MAX + g * 16));
    m = fmaxf(fmaxf(mm.x, mm.y), fmaxf(mm.z, mm.w));
  }
  float l = 0.f;
#pragma unroll
  for (int i = 0; i < 8; ++i) { sv[i] = __builtin_amdgcn_exp2f((sv[i] - m) * LOG2E); l += sv[i]; }
  asm volatile("" : "+v"(l));
  SLOT_SAME(QPOS + 9);
  l = row_sum(l);
  if (c == 0) lds_w32(L_SUM + g * 16 + wave * 4, asu(l));
  const u32 pw = L_P + wave * 1280;
  lds_w128(pw + g * 320 + c * 16, u32x4{pk(sv[0], sv[1]), pk(sv[2], sv[3]), pk(sv[4], sv[5]), pk(sv[6], sv[7])});
  FENCE();
  f32x4 acc[2] = {{0.f, 0.f, 0.f, 0.f}, {0.f, 0.f, 0.f, 0.f}};
  u32x4 am[4];
#define PV_A(u) do { _Pragma("unroll") for (int t = 0; t < 4; ++t) am[t] = lds_r128((((c & 3) == t) ? pw + (c >> 2) * 320 + g * 16 : L_ZERO) + (u) * 64); FENCE(); } while (0)
#define PV(u) do { tile_mma(am, v[u], acc); asm volatile("" : "+v"(acc[0]), "+v"(acc[1])); FENCE(); } while (0)
  PV_A(0);
  SLOT_BUSY(QPOS + 10);
  PV(0); PV_A(1);
  SLOT_SAME(QPOS + 11);
  WG_BARRIER();
  {
    const f32x4 ls = asf4(lds_r128(L_SUM + g * 16));
    const float lt = (ls.x + ls.y) + (ls.z + ls.w);
    st_dev8(u32x2{min(asu(m), SENT - 1), min(asu(lt), SENT - 1)}, rs_ws, (wave == 0 && c == 0) ? g * 128 : OOB, WS_ML + ga * 512 + gi * 8);
    FENCE();
  }
  constexpr int N1 = QPOS + 13 > PV_LAG + 16 ? QPOS + 13 : PV_LAG + 16, N2 = PV_LAG + 24;
  static_assert(N1 < N2 && N2 <= 32 + WO_PACED, "stream order");
  SLOTS(QPOS + 12, N1);
  PV(1); PV_A(2);
  SLOTS(N1, N2);
  PV(2); PV_A(3);
  SLOTS(N2, 32 + WO_PACED);
  const u32 ml_vo = (g * 4 + (c & 3)) * 8, ml_so = WS_ML + gp * 512 + ph * 128;
  u32x2 ml = ld_dev8(rs_ws, ml_vo, ml_so);
#pragma unroll
  for (int n = WO_PACED; n < 8; ++n) wo[n] = ld_stream(rs_wo, g * (8 * 1024) + c * 16, n * 1024);
  u32 mw = wave * 2048 + lane * 16, mr1 = L_MRG1 + (wave * 16 + c) * 16, ost_vo = g == 0 ? c * 16 : OOB, op_vo = c * 1024 + g * 16;
  u32 ost_so = WS_OP + cua * 1024 + wave * 256, op_so = WS_OP + gp * 16384 + ph * 256 + wave * 64;
  const u32 ln = (wave & 1) * 32 + (lane >> 1);
  u32 mr2 = L_MRG2 + (lane & 1) * 1024 + ln * 16, fst_vo = wave < 2 ? ln * 2048 + (lane & 1) * 16 : OOB;
  u32 fst_so = WS_FP + pq * (64 * 2048) + (gp * 4 + ph) * 32;
  asm volatile("" : "+v"(mw), "+v"(mr1), "+v"(ost_vo), "+v"(op_vo), "+v"(mr2), "+v"(fst_vo), "+s"(ost_so), "+s"(op_so), "+s"(fst_so));
  FENCE();
  {
    u32x4 b8[8];
#pragma unroll
    for (int d = 0; d < 4; ++d) {
#pragma unroll
      for (int j = 0; j < 8; ++j) b8[j][d] = __builtin_amdgcn_perm(v[3][2 * d + 1][j >> 1], v[3][2 * d][j >> 1], (j & 1) ? 0x07060302u : 0x05040100u);
      if (d == 2) { asm volatile("" : "+v"(b8[0]), "+v"(b8[1]), "+v"(b8[2]), "+v"(b8[3]), "+v"(b8[4]), "+v"(b8[5]), "+v"(b8[6]), "+v"(b8[7])); FENCE(); }
    }
#pragma unroll
    for (int t = 0; t < 4; ++t) { acc[0] = mfma(am[t], b8[t], acc[0]); acc[1] = mfma(am[t], b8[4 + t], acc[1]); }
  }
  lds_w128(L_MRG1 + mw, asu4(acc[0]));
  lds_w128(L_MRG1 + mw + 1024, asu4(acc[1]));
  WG_BARRIER();
  {
    u32x4 r[8];
#pragma unroll
    for (int i = 0; i < 8; ++i) r[i] = lds_r128(mr1 + i * 1024);
    FENCE();
    const f32x4 o0 = (asf4(r[0]) + asf4(r[2])) + (asf4(r[4]) + asf4(r[6])), o1 = (asf4(r[1]) + asf4(r[3])) + (asf4(r[5]) + asf4(r[7]));
    st_dev(clean(u32x4{pk(o0.x, o0.y), pk(o0.z, o0.w), pk(o1.x, o1.y), pk(o1.z, o1.w)}), rs_ws, ost_vo, ost_so);
  }
  FENCE();
  asm volatile("" : "+v"(ml));
  if (!__all(max(ml.x, ml.y) != SENT)) {
    for (u32 spin = 0; spin < SPIN_CAP; ++spin) {
      asm volatile("" ::: "memory");
      ml = ld_dev8(rs_ws, ml_vo, ml_so);
      if (__all(max(ml.x, ml.y) != SENT)) break;
    }
  }
  f32x2 f01, f23;
  {
    const float ms = asf(ml.x), ls = asf(ml.y);
    float M = fmaxf(ms, dpp<XOR1>(ms)); M = fmaxf(M, dpp<XOR2>(M));
    const float e = __builtin_amdgcn_exp2f((ms - M) * LOG2E);
    float L = e * ls; L += dpp<XOR1>(L); L += dpp<XOR2>(L);
    const float f = e / L;
    f01 = f32x2{dpp<0x00>(f), dpp<0x55>(f)}; f23 = f32x2{dpp<0xAA>(f), dpp<0xFF>(f)};
  }
  asm volatile("" : "+v"(f01), "+v"(f23));
  VMWAIT(1);
  u32x4 oa = ld_dev(rs_ws, op_vo, op_so);
  FENCE();
  u32x4 bf[8] = {bfrag<0>(wo), bfrag<1>(wo), bfrag<2>(wo), bfrag<3>(wo), bfrag<4>(wo), bfrag<5>(wo), bfrag<6>(wo), bfrag<7>(wo)};
  asm volatile("" : "+v"(bf[0]), "+v"(bf[1]), "+v"(bf[2]), "+v"(bf[3]), "+v"(bf[4]), "+v"(bf[5]), "+v"(bf[6]), "+v"(bf[7]));
  FENCE();
  {
    u32 budget = SPIN_CAP, t_;
    asm volatile(".Lgm_p1:\n\ts_waitcnt vmcnt(0)\n\tv_max3_u32 %1, v200, v201, v202\n\tv_max_u32 %1, %1, v203\n\tv_cmp_eq_u32 vcc, -1, %1\n\t"
                 "s_cbranch_vccz .Lgm_p1x\n\ts_sub_u32 %2, %2, 1\n\ts_cbranch_scc1 .Lgm_p1x\n\t"
                 "buffer_load_dwordx4 v[200:203], %3, %4, %5 offen sc1\n\ts_branch .Lgm_p1\n.Lgm_p1x:"
                 : "+{v[200:203]}"(oa), "=&v"(t_), "+s"(budget) : "v"(op_vo), "s"(rs_ws), "s"(op_so) : "vcc", "scc", "memory");
  }
  FENCE();
  {
    f32x4 d[8], y[2];
#pragma unroll
    for (int j = 0; j < 8; ++j) d[j] = mfma(oa, bf[j], f32x4{0.f, 0.f, 0.f, 0.f});
#pragma unroll
    for (int j = 0; j < 8; ++j) {
      const f32x2 t = __builtin_elementwise_fma(f23, f32x2{d[j].z, d[j].w}, f01 * f32x2{d[j].x, d[j].y});
      float yy = t.x + t.y;
      asm volatile("" : "+v"(yy));
      y[j >> 2][j & 3] = yy;
    }
    lds_w128(L_MRG2 + mw, asu4(y[0]));
    lds_w128(L_MRG2 + mw + 1024, asu4(y[1]));
    __builtin_amdgcn_sched_group_barrier(0x8, 4, 0);
#pragma unroll
    for (int j = 0; j < 4; ++j) { __builtin_amdgcn_sched_group_barrier(0x2, 3, 0); __builtin_amdgcn_sched_group_barrier(0x8, 1, 0); }
    __builtin_amdgcn_sched_group_barrier(0x200, 1, 0);
    __builtin_amdgcn_sched_group_barrier(0x2, 12, 0);
    __builtin_amdgcn_sched_group_barrier(0x200, 1, 0);
  }
  WG_BARRIER();
  {
    const f32x4 o = (asf4(lds_r128(mr2)) + asf4(lds_r128(mr2 + 2048))) + (asf4(lds_r128(mr2 + 4096)) + asf4(lds_r128(mr2 + 6144)));
    st_dev(clean(asu4(o)), rs_ws, fst_vo, fst_so);
  }
  FENCE();
  if (wave == 3) {
    const u32 fp_vo = (lane & 31) * 32 + (lane >> 5) * 16, fp_so = WS_FP + red * 2048;
    const float gt = asf(lds_r32(L_GATE));
    float gn = rbf(1.f - gt);
    asm volatile("" : "+v"(gn));
    float coef = (lane & 1) ? gn : gt;
    rsrc_t rs_out = mk(out, 4096);
    u32 out_vo = (lane & 25) == 0 ? (lane >> 5) * 8 + (lane & 2) * 2 + ((lane >> 2) & 1) * 2 : OOB;
    u32 out_so = ((red >> 4) & 3) * 1024 + (red >> 6) * 256 + (red & 15) * 16;
    u64 m0 = 0xAAAAAAAAAAAAAAAAull, m1 = 0xCCCCCCCCCCCCCCCCull, m2 = 0xF0F0F0F0F0F0F0F0ull;
    asm volatile("" : "+v"(coef), "+v"(out_vo), "+s"(out_so), "+s"(m0), "+s"(m1), "+s"(m2), "+s"(rs_out));
    __builtin_amdgcn_s_sleep(FP_HOLD);
    u32x4 pa = ld_dev(rs_ws, fp_vo, fp_so), pb = ld_dev(rs_ws, fp_vo + 1024, fp_so);
    {
      u32 budget = SPIN_CAP, t_, u_;
      asm volatile(".Lgm_p2:\n\ts_waitcnt vmcnt(1)\n\tv_max3_u32 %2, v200, v201, v202\n\ts_waitcnt vmcnt(0)\n\tv_max3_u32 %3, v204, v205, v206\n\t"
                   "v_max3_u32 %2, %2, v203, v207\n\tv_max_u32 %2, %2, %3\n\tv_cmp_eq_u32 vcc, -1, %2\n\t"
                   "s_cbranch_vccz .Lgm_p2x\n\ts_sub_u32 %4, %4, 1\n\ts_cbranch_scc1 .Lgm_p2x\n\t"
                   "buffer_load_dwordx4 v[200:203], %5, %6, %7 offen sc1\n\tbuffer_load_dwordx4 v[204:207], %5, %6, %7 offen offset:1024 sc1\n\t"
                   "s_branch .Lgm_p2\n.Lgm_p2x:"
                   : "+{v[200:203]}"(pa), "+{v[204:207]}"(pb), "=&v"(t_), "=&v"(u_), "+s"(budget) : "v"(fp_vo), "s"(rs_ws), "s"(fp_so) : "vcc", "scc", "memory");
    }
    FENCE();
    const bool b0 = __builtin_amdgcn_inverse_ballot_w64(m0), b1 = __builtin_amdgcn_inverse_ballot_w64(m1), b2 = __builtin_amdgcn_inverse_ballot_w64(m2);
    f32x4 k4;
#pragma unroll
    for (int i = 0; i < 4; ++i) k4[i] = asf(b0 ? pb[i] : pa[i]) + dpp<XOR1>(asf(b0 ? pa[i] : pb[i]));
    const float k0 = (b1 ? k4.z : k4.x) + dpp<XOR2>(b1 ? k4.x : k4.z), k1 = (b1 ? k4.w : k4.y) + dpp<XOR2>(b1 ? k4.y : k4.w);
    float s = (b2 ? k1 : k0) + dpp<XOR7>(dpp<XOR3>(b2 ? k0 : k1));
    s += dpp<ROR8>(s);
    s = x16_sum(s);
    const float t = rbf(coef * rbf(s));
    const float y = t + dpp<XOR1>(t);
    __builtin_amdgcn_raw_buffer_store_b16((unsigned short)pk(y, y), rs_out, out_vo, out_so, 0);
  }
  asm volatile(".Lgm_last:");
}

extern "C" void kernel_launch(void* const* d_in, const int* in_sizes, int n_in, void* d_out, int out_size, void* d_ws, size_t ws_size,
                              hipStream_t stream) {
  (void)in_sizes; (void)n_in; (void)out_size; (void)ws_size;
  (void)hipFuncSetAttribute((const void*)gated_mix_decode_kernel, hipFuncAttributeMaxDynamicSharedMemorySize, (int)L_END);
  hipLaunchKernelGGL(gated_mix_decode_kernel, dim3(256), dim3(320), L_END, stream, (const char*)d_in[3], (const char*)d_in[8],
                     (const char*)d_in[0], (char*)d_ws, (const char*)d_in[1], (const char*)d_in[6], (const char*)d_in[10],
                     (const char*)d_in[2], (const char*)d_in[7], (const char*)d_in[4], (const char*)d_in[9], (char*)d_out);
}
```
